```python
import torch

def _softmax(x, axis=-1):
    return torch.softmax(x, dim=axis)
def _gelu(x):
    return torch.nn.functional.gelu(x, approximate="tanh")
def _silu(x):
    return torch.nn.functional.silu(x)
def _sigmoid(x):
    return torch.sigmoid(x)
def _layernorm(x, axis=-1, eps=1e-5):
    m = x.mean(dim=axis, keepdim=True)
    v = x.var(dim=axis, keepdim=True, unbiased=False)
    return (x - m) / torch.sqrt(v + eps)
def _scatter_add(tgt, idx, src, axis=0):
    return tgt.clone().index_add_(axis, idx, src)
def _segment_sum(src, seg, n):
    out = torch.zeros((n,) + src.shape[1:], dtype=src.dtype)
    return out.index_add_(0, seg, src)
def _pad(x, pw):
    flat = tuple(v for pair in reversed(pw) for v in pair)
    return torch.nn.functional.pad(x, flat)

kernel_name = 'standalone__mha_softmax__near_ridge'

def setup_inputs():

    _w = (lambda a: (lambda t: t.to(torch.bfloat16) if t.is_floating_point() else t)(torch.as_tensor(a)))
    B, H, Sq, Sk, D = 8, 16, 256, 512, 256
    return {
        "q": _w((lambda *s: torch.randn(*s, dtype=torch.bfloat16) * 0.8)(B, H, Sq, D)),
        "k": _w((lambda *s: torch.randn(*s, dtype=torch.bfloat16) * 0.8)(B, H, Sk, D)),
        "v": _w((lambda *s: torch.randn(*s, dtype=torch.bfloat16) * 0.05)(B, H, Sk, D)),
    }

def reference(q, k, v):
    scale = q.shape[-1] ** -0.5
    scores = torch.einsum("bhqd,bhkd->bhqk", q, k) * scale
    p = _softmax(scores, axis=-1)
    return torch.einsum("bhqk,bhkd->bhqd", p, v)
```

```cpp
#include <hip/hip_runtime.h>
#include <hip/hip_bf16.h>
constexpr int SQ=256, SK=512, D=256;
__device__ __forceinline__ float bf2f(unsigned short x){ return __uint_as_float(((unsigned)x)<<16); }
__global__ __launch_bounds__(256) void naive_k(const unsigned short* q,const unsigned short* k,const unsigned short* v, __hip_bfloat16* o){
  __shared__ float qs[8][D]; __shared__ float p[8][SK]; __shared__ float red[8];
  int bh = blockIdx.x / (SQ/8), q0 = (blockIdx.x % (SQ/8))*8; int t=threadIdx.x;
  for(int r=0;r<8;++r) qs[r][t]=bf2f(q[((long)bh*SQ+q0+r)*D+t]);
  __syncthreads();
  for(int kk=t;kk<SK;kk+=256){ float acc[8]={0,0,0,0,0,0,0,0}; const unsigned short* kr=k+((long)bh*SK+kk)*D;
    for(int d=0;d<D;++d){ float kv=bf2f(kr[d]); for(int r=0;r<8;++r) acc[r]+=qs[r][d]*kv; }
    for(int r=0;r<8;++r) p[r][kk]=acc[r]*0.0625f; }
  __syncthreads();
  if(t<8){ float m=-1e30f; for(int i=0;i<SK;++i) m=fmaxf(m,p[t][i]); float s=0; for(int i=0;i<SK;++i){ float e=__expf(p[t][i]-m); p[t][i]=e; s+=e;} red[t]=1.f/s; }
  __syncthreads();
  float acc[8]={0,0,0,0,0,0,0,0};
  for(int kk=0;kk<SK;++kk){ float vv=bf2f(v[((long)bh*SK+kk)*D+t]); for(int r=0;r<8;++r) acc[r]+=p[r][kk]*vv; }
  for(int r=0;r<8;++r) o[((long)bh*SQ+q0+r)*D+t]=__float2bfloat16(acc[r]*red[r]);
}
extern "C" void kernel_launch(void* const* d_in, const int* in_sizes, int n_in, void* d_out, int out_size, void* d_ws, size_t ws_size, hipStream_t stream){
  naive_k<<<128*(SQ/8),256,0,stream>>>((const unsigned short*)d_in[0],(const unsigned short*)d_in[1],(const unsigned short*)d_in[2],(__hip_bfloat16*)d_out);
}
```

```cpp
#include <hip/hip_runtime.h>
#include <hip/hip_bf16.h>

__global__ __launch_bounds__(256, 1) void standalone__mha_softmax__near_ridge_kernel(const void* q, const void* k, const void* v, void* o) {
  __shared__ __attribute__((aligned(1024))) char lds[163840];
  unsigned wave = __builtin_amdgcn_readfirstlane(threadIdx.x >> 6), bid = blockIdx.x;
  unsigned long long ka = (unsigned long long)__builtin_amdgcn_kernarg_segment_ptr();
  asm volatile(
    "s_load_dwordx8 s[8:15], %0, 0x0\n"
    "v_mbcnt_lo_u32_b32 v217, -1, 0\n"
    "v_mbcnt_hi_u32_b32 v217, -1, v217\n"
    "s_mov_b32 s32, %2\n"
    "s_getpc_b64 s[62:63]\nCWA_%=:\n"
    "s_lshr_b32 s60, %1, 3\n"
    "s_lshl_b32 s60, s60, 2\n"
    "s_add_u32 s60, s60, s32\n"
    "s_mul_i32 s60, s60, 256\n"
    "s_and_b32 s60, s60, 0x7fff\n"
    "v_and_b32 v206, 1, v217\n"
    "v_lshl_add_u32 v206, v206, 7, s60\n"
    "v_min_u32 v206, CODEEND_%=-CWA_%=-4, v206\n"
    "global_load_dword v205, v206, s[62:63]\n"
    "s_lshl_b32 s33, s32, 12\n"
    "s_and_b32 s36, %1, 7\n"
    "s_lshr_b32 s37, %1, 3\n"
    "s_lshl_b32 s36, s36, 5\n"
    "s_add_u32 s36, s36, s37\n"
    "s_lshr_b32 s37, s36, 1\n"
    "v_bfe_u32 v208, v217, 2, 3\n"
    "v_lshlrev_b32 v208, 9, v208\n"
    "v_lshrrev_b32 v209, 5, v217\n"
    "v_lshl_add_u32 v208, v209, 6, v208\n"
    "v_and_b32 v209, 3, v217\n"
    "v_lshl_add_u32 v208, v209, 4, v208\n"
    "v_add_u32 v192, 0x1000, v208\n"
    "v_subrev_u32 v193, 0x380, v192\n"
    "v_subrev_u32 v194, 0x700, v192\n"
    "v_subrev_u32 v195, 0xa80, v192\n"
    "v_xor_b32 v196, 48, v192\n"
    "v_xor_b32 v197, 48, v193\n"
    "v_xor_b32 v198, 48, v194\n"
    "v_xor_b32 v199, 48, v195\n"
    "s_and_b32 s62, s32, 1\n"
    "s_mul_i32 s62, s62, 48\n"
    "v_xor_b32 v224, s62, v192\n"
    "v_xor_b32 v225, s62, v193\n"
    "v_xor_b32 v226, s62, v194\n"
    "v_xor_b32 v227, s62, v195\n"
    "s_lshl_b32 s52, s36, 16\n"
    "s_lshl_b32 s53, s32, 14\n"
    "s_add_u32 s52, s52, s53\n"
    "s_lshl_b32 s54, s37, 18\n"
    "s_sub_u32 s55, s52, 0x1000\n"
    "s_ashr_i32 s56, s55, 31\n"
    "s_add_u32 s57, s54, s33\n"
    "s_sub_u32 s57, s57, 0x1000\n"
    "s_ashr_i32 s58, s57, 31\n"
    "s_mov_b32 s18, 0x5000\n"
    "s_mov_b32 s19, 0x20000\n"
    "s_sub_u32 s22, 0x41000, s33\n"
    "s_mov_b32 s23, 0x20000\n"
    "s_add_u32 s61, s33, 0x18000\n"
    "s_mov_b32 m0, s61\n"
    "s_mov_b32 s36, 0x1000\n"
    "s_mov_b32 s37, 0x2000\n"
    "s_mov_b32 s60, 0x3000\n"
    "s_mov_b32 s64, 0x0\n"
    "s_mov_b32 s65, 0x4000\n"
    "s_mov_b32 s66, 0x8000\n"
    "s_mov_b32 s67, 0xc000\n"
    "s_waitcnt lgkmcnt(0)\n"
    "s_add_u32 s16, s8, s55\n"
    "s_addc_u32 s17, s9, s56\n"
    "s_and_b32 s17, s17, 0xffff\n"
    "buffer_load_dwordx4 v192, s[16:19], 0 offen offset:0 nt lds\n"
    "s_add_u32 m0, s61, 0x400\n"
    "s_nop 0\n"
    "buffer_load_dwordx4 v196, s[16:19], s36 offen offset:0 nt lds\n"
    "s_add_u32 m0, s61, 0x800\n"
    "s_add_u32 s55, s54, s53\n"
    "s_add_u32 s24, s12, s55\n"
    "s_addc_u32 s25, s13, 0\n"
    "s_and_b32 s25, s25, 0xffff\n"
    "s_sub_u32 s26, 0x40000, s53\n"
    "s_mov_b32 s27, 0x20000\n"
    "buffer_load_dwordx4 v192, s[16:19], s37 offen offset:0 nt lds\n"
    "s_add_u32 m0, s61, 0xc00\n"
    "s_add_u32 s28, s14, s52\n"
    "s_addc_u32 s29, s15, 0\n"
    "s_and_b32 s29, s29, 0xffff\n"
    "s_mov_b32 s30, 0x4000\n"
    "s_mov_b32 s31, 0x20000\n"
    "s_mov_b32 s35, 0x3db8aa3b\n"
    "buffer_load_dwordx4 v196, s[16:19], s60 offen offset:0 nt lds\n"
    "s_mov_b32 s48, 0x81ff81ff\n"
    "v_mov_b32 v207, 0x3e003e00\n"
    "v_mul_u32_u24 v234, 0x9e3779, v217\n"
    "v_alignbit_b32 v234, v234, v234, 7\n"
    "v_xor_b32 v234, 0xdaa66d13, v234\n"
    "v_and_or_b32 v0, v234, s48, v207\n"
    "v_alignbit_b32 v234, v234, v234, 8\n"
    "v_xor_b32 v234, 0x78dde6c4, v234\n"
    "v_and_or_b32 v1, v234, s48, v207\n"
    "v_alignbit_b32 v234, v234, v234, 9\n"
    "v_xor_b32 v234, 0x17156075, v234\n"
    "v_and_or_b32 v2, v234, s48, v207\n"
    "v_alignbit_b32 v234, v234, v234, 10\n"
    "v_xor_b32 v234, 0xb54cda26, v234\n"
    "v_and_or_b32 v3, v234, s48, v207\n"
    "v_alignbit_b32 v234, v234, v234, 11\n"
    "v_xor_b32 v234, 0x538453d7, v234\n"
    "v_and_or_b32 v4, v234, s48, v207\n"
    "v_alignbit_b32 v234, v234, v234, 7\n"
    "v_xor_b32 v234, 0xf1bbcd88, v234\n"
    "v_and_or_b32 v5, v234, s48, v207\n"
    "v_alignbit_b32 v234, v234, v234, 8\n"
    "v_xor_b32 v234, 0x8ff34739, v234\n"
    "v_and_or_b32 v6, v234, s48, v207\n"
    "v_alignbit_b32 v234, v234, v234, 9\n"
    "v_xor_b32 v234, 0x2e2ac0ea, v234\n"
    "v_and_or_b32 v7, v234, s48, v207\n"
    "s_nop 1\n"
    "v_mfma_f32_32x32x16_bf16 v[64:79], v[0:3], v[4:7], 0\n"
    "v_alignbit_b32 v234, v234, v234, 10\n"
    "v_xor_b32 v234, 0xcc623a9b, v234\n"
    "v_and_or_b32 v8, v234, s48, v207\n"
    "v_alignbit_b32 v234, v234, v234, 11\n"
    "v_xor_b32 v234, 0x6a99b44c, v234\n"
    "v_and_or_b32 v9, v234, s48, v207\n"
    "v_alignbit_b32 v234, v234, v234, 7\n"
    "v_xor_b32 v234, 0x8d12dfd, v234\n"
    "v_and_or_b32 v10, v234, s48, v207\n"
    "v_alignbit_b32 v234, v234, v234, 8\n"
    "v_xor_b32 v234, 0xa708a7ae, v234\n"
    "v_and_or_b32 v11, v234, s48, v207\n"
    "v_alignbit_b32 v234, v234, v234, 9\n"
    "v_xor_b32 v234, 0x4540215f, v234\n"
    "v_and_or_b32 v12, v234, s48, v207\n"
    "v_alignbit_b32 v234, v234, v234, 10\n"
    "v_xor_b32 v234, 0xe3779b10, v234\n"
    "v_and_or_b32 v13, v234, s48, v207\n"
    "v_alignbit_b32 v234, v234, v234, 11\n"
    "v_xor_b32 v234, 0x81af14c1, v234\n"
    "v_and_or_b32 v14, v234, s48, v207\n"
    "v_alignbit_b32 v234, v234, v234, 7\n"
    "v_xor_b32 v234, 0x1fe68e72, v234\n"
    "v_and_or_b32 v15, v234, s48, v207\n"
    "s_add_u32 m0, s33, 0x0\n"
    "s_add_u32 s20, s10, s57\n"
    "s_addc_u32 s21, s11, s58\n"
    "s_and_b32 s21, s21, 0xffff\n"
    "buffer_load_dwordx4 v224, s[20:23], s64 offen offset:0 nt lds\n"
    "s_add_u32 m0, s33, 0x4000\n"
    "v_mfma_f32_32x32x16_bf16 v[64:79], v[0:3], v[4:7], v[64:79]\n"
    "v_bfe_u32 v210, v217, 3, 1\n"
    "v_and_b32 v209, 7, v217\n"
    "v_lshlrev_b32 v208, 6, v209\n"
    "v_mul_u32_u24 v211, 3, v210\n"
    "v_lshrrev_b32 v209, 4, v217\n"
    "v_xor_b32 v209, v209, v211\n"
    "v_lshl_add_u32 v208, v209, 4, v208\n"
    "v_lshl_add_u32 v236, v210, 12, v208\n"
    "buffer_load_dwordx4 v224, s[20:23], s65 offen offset:0 nt lds\n"
    "s_add_u32 m0, s33, 0x8000\n"
    "v_mfma_f32_32x32x16_bf16 v[64:79], v[4:7], v[12:15], v[64:79]\n"
    "v_lshl_add_u32 v208, v210, 10, v208\n"
    "v_add_u32 v235, s61, v208\n"
    "v_add_u32 v237, 0x10000, v236\n"
    "v_add_u32 v238, 0x20000, v236\n"
    "buffer_load_dwordx4 v224, s[20:23], s66 offen offset:0 nt lds\n"
    "s_add_u32 m0, s33, 0xc000\n"
    "v_mfma_f32_32x32x16_bf16 v[64:79], v[8:11], v[4:7], v[64:79]\n"
    "v_lshrrev_b32 v208, 3, v217\n"
    "v_and_b32 v209, 7, v217\n"
    "v_lshrrev_b32 v210, 4, v217\n"
    "v_xor_b32 v210, v210, v209\n"
    "v_lshlrev_b32 v211, 9, v208\n"
    "v_lshl_add_u32 v228, v210, 4, v211\n"
    "v_add_u32 v229, 0xc00, v228\n"
    "v_add_u32 v230, 0x1800, v228\n"
    "v_add_u32 v231, 0x2400, v228\n"
    "buffer_load_dwordx4 v224, s[20:23], s67 offen offset:0 nt lds\n"
    "s_add_u32 m0, s61, 0x3c00\n"
    "v_mfma_f32_32x32x16_bf16 v[64:79], v[12:15], v[12:15], v[64:79]\n"
    "v_bfe_u32 v208, v217, 2, 3\n"
    "v_lshlrev_b32 v208, 11, v208\n"
    "v_lshrrev_b32 v209, 5, v217\n"
    "v_lshl_add_u32 v208, v209, 6, v208\n"
    "v_and_b32 v209, 3, v217\n"
    "v_lshl_add_u32 v232, v209, 4, v208\n"
    "s_mov_b32 s44, 0x55555555\n"
    "s_mov_b32 s45, 0x55555555\n"
    "s_mov_b32 s46, 0x33333333\n"
    "s_mov_b32 s47, 0x33333333\n"
    "buffer_load_dwordx4 v193, s[16:19], 0 offen offset:1024 nt lds\n"
    "s_add_u32 m0, s61, 0x4000\n"
    "v_mfma_f32_32x32x16_bf16 v[64:79], v[0:3], v[4:7], v[64:79]\n"
    "v_lshrrev_b32 v208, 2, v217\n"
    "v_lshlrev_b32 v208, 7, v208\n"
    "v_bfe_u32 v209, v217, 1, 1\n"
    "v_lshl_add_u32 v208, v209, 6, v208\n"
    "v_and_b32 v209, 1, v217\n"
    "v_lshl_add_u32 v208, v209, 3, v208\n"
    "v_mfma_f32_32x32x16_bf16 v[64:79], v[4:7], v[8:11], v[64:79]\n"
    "v_bfe_u32 v209, v217, 3, 2\n"
    "v_lshl_add_u32 v240, v209, 4, v208\n"
    "v_xor_b32 v241, 16, v240\n"
    "v_xor_b32 v242, 32, v240\n"
    "v_xor_b32 v243, 48, v240\n"
    "buffer_load_dwordx4 v197, s[16:19], s36 offen offset:1024 nt lds\n"
    "s_add_u32 m0, s61, 0x4400\n"
    "v_mfma_f32_32x32x16_bf16 v[64:79], v[4:7], v[12:15], v[64:79]\n"
    "v_add_u32 v244, 0x10000, v240\n"
    "v_add_u32 v245, 0x10000, v241\n"
    "v_add_u32 v246, 0x10000, v242\n"
    "v_add_u32 v247, 0x10000, v243\n"
    "v_mfma_f32_32x32x16_bf16 v[64:79], v[8:11], v[0:3], v[64:79]\n"
    "v_add_u32 v248, 0x20000, v240\n"
    "v_add_u32 v249, 0x20000, v241\n"
    "v_add_u32 v250, 0x20000, v242\n"
    "v_add_u32 v251, 0x20000, v243\n"
    "buffer_load_dwordx4 v193, s[16:19], s37 offen offset:1024 nt lds\n"
    "s_add_u32 m0, s61, 0x4800\n"
    "v_mfma_f32_32x32x16_bf16 v[64:79], v[8:11], v[4:7], v[64:79]\n"
    "v_mov_b32 v200, 0x3f803f80\n"
    "v_mov_b32 v201, 0x3f803f80\n"
    "v_mfma_f32_32x32x16_bf16 v[64:79], v[12:15], v[8:11], v[64:79]\n"
    "v_mov_b32 v202, 0x3f803f80\n"
    "v_mov_b32 v203, 0x3f803f80\n"
    "buffer_load_dwordx4 v197, s[16:19], s60 offen offset:1024 nt lds\n"
    "s_add_u32 m0, s33, 0x0\n"
    "v_mfma_f32_32x32x16_bf16 v[64:79], v[12:15], v[12:15], v[64:79]\n"
    "v_mfma_f32_32x32x16_bf16 v[64:79], v[0:3], v[0:3], v[64:79]\n"
    "s_mov_b32 s68, 0x10000\n"
    "s_mov_b32 s69, 0x14000\n"
    "s_mov_b32 s70, 0x18000\n"
    "s_mov_b32 s71, 0x1c000\n"
    "s_mov_b32 s72, 0x20000\n"
    "s_mov_b32 s73, 0x24000\n"
    "s_mov_b32 s74, 0x28000\n"
    "buffer_load_dwordx4 v225, s[20:23], s64 offen offset:1024 nt lds\n"
    "s_add_u32 m0, s33, 0x4000\n"
    "v_mfma_f32_32x32x16_bf16 v[64:79], v[0:3], v[4:7], v[64:79]\n"
    "v_mfma_f32_32x32x16_bf16 v[64:79], v[4:7], v[8:11], v[64:79]\n"
    "s_mov_b32 s75, 0x2c000\n"
    "s_mov_b32 s76, 0x30000\n"
    "s_mov_b32 s77, 0x34000\n"
    "s_mov_b32 s78, 0x38000\n"
    "s_mov_b32 s79, 0x3c000\n"
    "s_mov_b32 s80, 0x0\n"
    "s_mov_b32 s81, 0x10000\n"
    "buffer_load_dwordx4 v225, s[20:23], s65 offen offset:1024 nt lds\n"
    "s_add_u32 m0, s33, 0x8000\n"
    "v_mfma_f32_32x32x16_bf16 v[64:79], v[4:7], v[12:15], v[64:79]\n"
    "v_mfma_f32_32x32x16_bf16 v[64:79], v[8:11], v[0:3], v[64:79]\n"
    "s_mov_b32 s82, 0x20000\n"
    "s_mov_b32 s83, 0x30000\n"
    "s_mov_b32 s84, 0x80\n"
    "s_mov_b32 s85, 0x10080\n"
    "s_mov_b32 s86, 0x20080\n"
    "s_mov_b32 s87, 0x30080\n"
    "s_mov_b32 s88, 0x100\n"
    "buffer_load_dwordx4 v225, s[20:23], s66 offen offset:1024 nt lds\n"
    "s_add_u32 m0, s33, 0xc000\n"
    "s_mov_b64 exec, 0\n"
    "s_cmp_eq_u32 s32, 0\n"
    "s_cbranch_scc1 PAD0_%=\n"
    "s_cmp_eq_u32 s32, 1\n"
    "s_cbranch_scc1 PAD1_%=\n"
    "s_cmp_eq_u32 s32, 2\n"
    "s_cbranch_scc1 PAD2_%=\n"
    "s_branch PAD3_%=\nPADRET_%=:\n"
    "s_mov_b64 exec, -1\n"
    "s_mov_b32 s89, 0x10100\n"
    "s_mov_b32 s90, 0x20100\n"
    "s_mov_b32 s91, 0x30100\n"
    "s_mov_b32 s92, 0x180\n"
    "s_mov_b32 s93, 0x10180\n"
    "s_mov_b32 s94, 0x20180\n"
    "s_mov_b32 s95, 0x30180\n"
    "buffer_load_dwordx4 v225, s[20:23], s67 offen offset:1024 nt lds\n"
    "v_mfma_f32_32x32x16_bf16 v[64:79], v[12:15], v[12:15], v[64:79]\n"
    "v_mfma_f32_32x32x16_bf16 v[64:79], v[0:3], v[0:3], v[64:79]\n"
    "s_waitcnt vmcnt(12)\n"
    "ds_read_b128 v[96:99], v235 offset:0\n"
    "ds_read_b128 v[100:103], v235 offset:512\n"
    "ds_read_b128 v[104:107], v235 offset:2048\n"
    "ds_read_b128 v[108:111], v235 offset:2560\n"
    "s_waitcnt lgkmcnt(3)\n"
    "v_lshlrev_b32 v92, 16, v96\n"
    "v_lshlrev_b32 v94, 16, v97\n"
    "v_and_b32 v93, 0xffff0000, v96\n"
    "v_and_b32 v95, 0xffff0000, v97\n"
    "v_mul_f32 v92, s35, v92\n"
    "v_mul_f32 v94, s35, v94\n"
    "v_mul_f32 v93, s35, v93\n"
    "v_mul_f32 v95, s35, v95\n"
    "v_cvt_pk_bf16_f32 v92, v92, v93\n"
    "v_cvt_pk_bf16_f32 v94, v94, v95\n"
    "v_accvgpr_write_b32 a0, v92\n"
    "v_accvgpr_write_b32 a1, v94\n"
    "v_lshlrev_b32 v92, 16, v98\n"
    "v_lshlrev_b32 v94, 16, v99\n"
    "v_and_b32 v93, 0xffff0000, v98\n"
    "v_and_b32 v95, 0xffff0000, v99\n"
    "v_mul_f32 v92, s35, v92\n"
    "v_mul_f32 v94, s35, v94\n"
    "v_mul_f32 v93, s35, v93\n"
    "v_mul_f32 v95, s35, v95\n"
    "v_cvt_pk_bf16_f32 v92, v92, v93\n"
    "v_cvt_pk_bf16_f32 v94, v94, v95\n"
    "v_accvgpr_write_b32 a2, v92\n"
    "v_accvgpr_write_b32 a3, v94\n"
    "s_waitcnt lgkmcnt(2)\n"
    "v_lshlrev_b32 v92, 16, v100\n"
    "v_lshlrev_b32 v94, 16, v101\n"
    "v_and_b32 v93, 0xffff0000, v100\n"
    "v_and_b32 v95, 0xffff0000, v101\n"
    "v_mul_f32 v92, s35, v92\n"
    "v_mul_f32 v94, s35, v94\n"
    "v_mul_f32 v93, s35, v93\n"
    "v_mul_f32 v95, s35, v95\n"
    "v_cvt_pk_bf16_f32 v92, v92, v93\n"
    "v_cvt_pk_bf16_f32 v94, v94, v95\n"
    "v_accvgpr_write_b32 a4, v92\n"
    "v_accvgpr_write_b32 a5, v94\n"
    "v_lshlrev_b32 v92, 16, v102\n"
    "v_lshlrev_b32 v94, 16, v103\n"
    "v_and_b32 v93, 0xffff0000, v102\n"
    "v_and_b32 v95, 0xffff0000, v103\n"
    "v_mul_f32 v92, s35, v92\n"
    "v_mul_f32 v94, s35, v94\n"
    "v_mul_f32 v93, s35, v93\n"
    "v_mul_f32 v95, s35, v95\n"
    "v_cvt_pk_bf16_f32 v92, v92, v93\n"
    "v_cvt_pk_bf16_f32 v94, v94, v95\n"
    "v_accvgpr_write_b32 a6, v92\n"
    "v_accvgpr_write_b32 a7, v94\n"
    "s_waitcnt lgkmcnt(0)\n"
    "v_lshlrev_b32 v92, 16, v104\n"
    "v_lshlrev_b32 v94, 16, v105\n"
    "v_and_b32 v93, 0xffff0000, v104\n"
    "v_and_b32 v95, 0xffff0000, v105\n"
    "v_mul_f32 v92, s35, v92\n"
    "v_mul_f32 v94, s35, v94\n"
    "v_mul_f32 v93, s35, v93\n"
    "v_mul_f32 v95, s35, v95\n"
    "v_cvt_pk_bf16_f32 v92, v92, v93\n"
    "v_cvt_pk_bf16_f32 v94, v94, v95\n"
    "v_accvgpr_write_b32 a32, v92\n"
    "v_accvgpr_write_b32 a33, v94\n"
    "v_lshlrev_b32 v92, 16, v106\n"
    "v_lshlrev_b32 v94, 16, v107\n"
    "v_and_b32 v93, 0xffff0000, v106\n"
    "v_and_b32 v95, 0xffff0000, v107\n"
    "v_mul_f32 v92, s35, v92\n"
    "v_mul_f32 v94, s35, v94\n"
    "v_mul_f32 v93, s35, v93\n"
    "v_mul_f32 v95, s35, v95\n"
    "v_cvt_pk_bf16_f32 v92, v92, v93\n"
    "v_cvt_pk_bf16_f32 v94, v94, v95\n"
    "v_accvgpr_write_b32 a34, v92\n"
    "v_accvgpr_write_b32 a35, v94\n"
    "v_lshlrev_b32 v92, 16, v108\n"
    "v_lshlrev_b32 v94, 16, v109\n"
    "v_and_b32 v93, 0xffff0000, v108\n"
    "v_and_b32 v95, 0xffff0000, v109\n"
    "v_mul_f32 v92, s35, v92\n"
    "v_mul_f32 v94, s35, v94\n"
    "v_mul_f32 v93, s35, v93\n"
    "v_mul_f32 v95, s35, v95\n"
    "v_cvt_pk_bf16_f32 v92, v92, v93\n"
    "v_cvt_pk_bf16_f32 v94, v94, v95\n"
    "v_accvgpr_write_b32 a36, v92\n"
    "v_accvgpr_write_b32 a37, v94\n"
    "v_lshlrev_b32 v92, 16, v110\n"
    "v_lshlrev_b32 v94, 16, v111\n"
    "v_and_b32 v93, 0xffff0000, v110\n"
    "v_and_b32 v95, 0xffff0000, v111\n"
    "v_mul_f32 v92, s35, v92\n"
    "v_mul_f32 v94, s35, v94\n"
    "v_mul_f32 v93, s35, v93\n"
    "v_mul_f32 v95, s35, v95\n"
    "v_cvt_pk_bf16_f32 v92, v92, v93\n"
    "v_cvt_pk_bf16_f32 v94, v94, v95\n"
    "v_accvgpr_write_b32 a38, v92\n"
    "v_accvgpr_write_b32 a39, v94\n"
    "s_waitcnt vmcnt(8) lgkmcnt(0)\n"
    "s_barrier\n"
    "ds_read_b128 a[64:67], v236 offset:0\n"
    "ds_read_b128 a[68:71], v236 offset:512\n"
    "ds_read_b128 a[72:75], v236 offset:8192\n"
    "ds_read_b128 a[76:79], v236 offset:8704\n"
    "ds_read_b128 a[80:83], v236 offset:16384\n"
    "ds_read_b128 a[84:87], v236 offset:16896\n"
    "s_add_u32 m0, s61, 0x7800\n"
    "s_nop 0\n"
    "buffer_load_dwordx4 v194, s[16:19], 0 offen offset:2048 nt lds\n"
    "s_waitcnt lgkmcnt(5)\n"
    "v_mfma_f32_16x16x32_bf16 v[128:131], a[64:67], a[0:3], 0\n"
    "ds_read_b128 a[88:91], v236 offset:24576\n"
    "s_nop 1\n"
    "s_waitcnt lgkmcnt(5)\n"
    "v_mfma_f32_16x16x32_bf16 v[128:131], a[68:71], a[4:7], v[128:131]\n"
    "s_nop 1\n"
    "v_mfma_f32_16x16x32_bf16 v[132:135], a[64:67], a[32:35], 0\n"
    "ds_read_b128 a[92:95], v236 offset:25088\n"
    "s_nop 1\n"
    "v_mfma_f32_16x16x32_bf16 v[132:135], a[68:71], a[36:39], v[132:135]\n"
    "s_nop 1\n"
    "s_waitcnt lgkmcnt(5)\n"
    "v_mfma_f32_16x16x32_bf16 v[136:139], a[72:75], a[0:3], 0\n"
    "ds_read_b128 a[96:99], v236 offset:32768\n"
    "s_nop 1\n"
    "s_waitcnt lgkmcnt(5)\n"
    "v_mfma_f32_16x16x32_bf16 v[136:139], a[76:79], a[4:7], v[136:139]\n"
    "s_add_u32 m0, s61, 0x7c00\n"
    "s_nop 0\n"
    "buffer_load_dwordx4 v198, s[16:19], s36 offen offset:2048 nt lds\n"
    "s_nop 1\n"
    "v_mfma_f32_16x16x32_bf16 v[140:143], a[72:75], a[32:35], 0\n"
    "ds_read_b128 a[100:103], v236 offset:33280\n"
    "s_nop 1\n"
    "v_mfma_f32_16x16x32_bf16 v[140:143], a[76:79], a[36:39], v[140:143]\n"
    "s_nop 1\n"
    "s_waitcnt lgkmcnt(3)\n"
    "v_mfma_f32_16x16x32_bf16 v[144:147], a[80:83], a[0:3], 0\n"
    "ds_read_b128 a[104:107], v236 offset:40960\n"
    "s_nop 1\n"
    "v_mfma_f32_16x16x32_bf16 v[144:147], a[84:87], a[4:7], v[144:147]\n"
    "s_nop 1\n"
    "v_mfma_f32_16x16x32_bf16 v[148:151], a[80:83], a[32:35], 0\n"
    "ds_read_b128 a[108:111], v236 offset:41472\n"
    "s_nop 1\n"
    "v_mfma_f32_16x16x32_bf16 v[148:151], a[84:87], a[36:39], v[148:151]\n"
    "s_nop 1\n"
    "v_mfma_f32_16x16x32_bf16 v[152:155], a[88:91], a[0:3], 0\n"
    "ds_read_b128 a[112:115], v236 offset:49152\n"
    "s_nop 1\n"
    "s_waitcnt lgkmcnt(4)\n"
    "v_mfma_f32_16x16x32_bf16 v[152:155], a[92:95], a[4:7], v[152:155]\n"
    "s_add_u32 m0, s61, 0x8000\n"
    "s_nop 0\n"
    "buffer_load_dwordx4 v194, s[16:19], s37 offen offset:2048 nt lds\n"
    "s_nop 1\n"
    "v_mfma_f32_16x16x32_bf16 v[156:159], a[88:91], a[32:35], 0\n"
    "ds_read_b128 a[116:119], v236 offset:49664\n"
    "s_nop 1\n"
    "v_mfma_f32_16x16x32_bf16 v[156:159], a[92:95], a[36:39], v[156:159]\n"
    "s_nop 1\n"
    "v_mfma_f32_16x16x32_bf16 v[160:163], a[96:99], a[0:3], 0\n"
    "ds_read_b128 a[120:123], v236 offset:57344\n"
    "s_nop 1\n"
    "s_waitcnt lgkmcnt(3)\n"
    "v_mfma_f32_16x16x32_bf16 v[160:163], a[100:103], a[4:7], v[160:163]\n"
    "s_nop 1\n"
    "v_mfma_f32_16x16x32_bf16 v[164:167], a[96:99], a[32:35], 0\n"
    "ds_read_b128 a[124:127], v236 offset:57856\n"
    "s_nop 1\n"
    "v_mfma_f32_16x16x32_bf16 v[164:167], a[100:103], a[36:39], v[164:167]\n"
    "s_nop 1\n"
    "v_mfma_f32_16x16x32_bf16 v[168:171], a[104:107], a[0:3], 0\n"
    "s_nop 1\n"
    "v_mfma_f32_16x16x32_bf16 v[168:171], a[108:111], a[4:7], v[168:171]\n"
    "s_add_u32 m0, s61, 0x8400\n"
    "s_nop 0\n"
    "buffer_load_dwordx4 v198, s[16:19], s60 offen offset:2048 nt lds\n"
    "s_nop 1\n"
    "v_mfma_f32_16x16x32_bf16 v[172:175], a[104:107], a[32:35], 0\n"
    "s_nop 1\n"
    "v_mfma_f32_16x16x32_bf16 v[172:175], a[108:111], a[36:39], v[172:175]\n"
    "s_nop 1\n"
    "s_waitcnt lgkmcnt(1)\n"
    "v_mfma_f32_16x16x32_bf16 v[176:179], a[112:115], a[0:3], 0\n"
    "s_nop 1\n"
    "v_mfma_f32_16x16x32_bf16 v[176:179], a[116:119], a[4:7], v[176:179]\n"
    "s_nop 1\n"
    "v_mfma_f32_16x16x32_bf16 v[180:183], a[112:115], a[32:35], 0\n"
    "s_nop 1\n"
    "v_mfma_f32_16x16x32_bf16 v[180:183], a[116:119], a[36:39], v[180:183]\n"
    "s_nop 1\n"
    "v_mfma_f32_16x16x32_bf16 v[184:187], a[120:123], a[0:3], 0\n"
    "s_nop 1\n"
    "s_waitcnt lgkmcnt(0)\n"
    "v_mfma_f32_16x16x32_bf16 v[184:187], a[124:127], a[4:7], v[184:187]\n"
    "s_add_u32 m0, s33, 0x0\n"
    "s_nop 0\n"
    "buffer_load_dwordx4 v226, s[20:23], s64 offen offset:2048 nt lds\n"
    "s_nop 1\n"
    "v_mfma_f32_16x16x32_bf16 v[188:191], a[120:123], a[32:35], 0\n"
    "s_nop 1\n"
    "v_mfma_f32_16x16x32_bf16 v[188:191], a[124:127], a[36:39], v[188:191]\n"
    "s_nop 1\n"
    "s_add_u32 m0, s33, 0x4000\n"
    "s_nop 0\n"
    "buffer_load_dwordx4 v226, s[20:23], s65 offen offset:2048 nt lds\n"
    "s_add_u32 m0, s33, 0x8000\n"
    "s_nop 0\n"
    "buffer_load_dwordx4 v226, s[20:23], s66 offen offset:2048 nt lds\n"
    "s_add_u32 m0, s33, 0xc000\n"
    "s_nop 0\n"
    "buffer_load_dwordx4 v226, s[20:23], s67 offen offset:2048 nt lds\n"
    "s_waitcnt vmcnt(12)\n"
    "ds_read_b128 v[96:99], v235 offset:16384\n"
    "ds_read_b128 v[100:103], v235 offset:16896\n"
    "ds_read_b128 v[104:107], v235 offset:18432\n"
    "ds_read_b128 v[108:111], v235 offset:18944\n"
    "s_waitcnt lgkmcnt(3)\n"
    "v_lshlrev_b32 v92, 16, v96\n"
    "v_lshlrev_b32 v94, 16, v97\n"
    "v_and_b32 v93, 0xffff0000, v96\n"
    "v_and_b32 v95, 0xffff0000, v97\n"
    "v_mul_f32 v92, s35, v92\n"
    "v_mul_f32 v94, s35, v94\n"
    "v_mul_f32 v93, s35, v93\n"
    "v_mul_f32 v95, s35, v95\n"
    "v_cvt_pk_bf16_f32 v92, v92, v93\n"
    "v_cvt_pk_bf16_f32 v94, v94, v95\n"
    "v_accvgpr_write_b32 a8, v92\n"
    "v_accvgpr_write_b32 a9, v94\n"
    "v_lshlrev_b32 v92, 16, v98\n"
    "v_lshlrev_b32 v94, 16, v99\n"
    "v_and_b32 v93, 0xffff0000, v98\n"
    "v_and_b32 v95, 0xffff0000, v99\n"
    "v_mul_f32 v92, s35, v92\n"
    "v_mul_f32 v94, s35, v94\n"
    "v_mul_f32 v93, s35, v93\n"
    "v_mul_f32 v95, s35, v95\n"
    "v_cvt_pk_bf16_f32 v92, v92, v93\n"
    "v_cvt_pk_bf16_f32 v94, v94, v95\n"
    "v_accvgpr_write_b32 a10, v92\n"
    "v_accvgpr_write_b32 a11, v94\n"
    "s_waitcnt lgkmcnt(2)\n"
    "v_lshlrev_b32 v92, 16, v100\n"
    "v_lshlrev_b32 v94, 16, v101\n"
    "v_and_b32 v93, 0xffff0000, v100\n"
    "v_and_b32 v95, 0xffff0000, v101\n"
    "v_mul_f32 v92, s35, v92\n"
    "v_mul_f32 v94, s35, v94\n"
    "v_mul_f32 v93, s35, v93\n"
    "v_mul_f32 v95, s35, v95\n"
    "v_cvt_pk_bf16_f32 v92, v92, v93\n"
    "v_cvt_pk_bf16_f32 v94, v94, v95\n"
    "v_accvgpr_write_b32 a12, v92\n"
    "v_accvgpr_write_b32 a13, v94\n"
    "v_lshlrev_b32 v92, 16, v102\n"
    "v_lshlrev_b32 v94, 16, v103\n"
    "v_and_b32 v93, 0xffff0000, v102\n"
    "v_and_b32 v95, 0xffff0000, v103\n"
    "v_mul_f32 v92, s35, v92\n"
    "v_mul_f32 v94, s35, v94\n"
    "v_mul_f32 v93, s35, v93\n"
    "v_mul_f32 v95, s35, v95\n"
    "v_cvt_pk_bf16_f32 v92, v92, v93\n"
    "v_cvt_pk_bf16_f32 v94, v94, v95\n"
    "v_accvgpr_write_b32 a14, v92\n"
    "v_accvgpr_write_b32 a15, v94\n"
    "s_waitcnt lgkmcnt(0)\n"
    "v_lshlrev_b32 v92, 16, v104\n"
    "v_lshlrev_b32 v94, 16, v105\n"
    "v_and_b32 v93, 0xffff0000, v104\n"
    "v_and_b32 v95, 0xffff0000, v105\n"
    "v_mul_f32 v92, s35, v92\n"
    "v_mul_f32 v94, s35, v94\n"
    "v_mul_f32 v93, s35, v93\n"
    "v_mul_f32 v95, s35, v95\n"
    "v_cvt_pk_bf16_f32 v92, v92, v93\n"
    "v_cvt_pk_bf16_f32 v94, v94, v95\n"
    "v_accvgpr_write_b32 a40, v92\n"
    "v_accvgpr_write_b32 a41, v94\n"
    "v_lshlrev_b32 v92, 16, v106\n"
    "v_lshlrev_b32 v94, 16, v107\n"
    "v_and_b32 v93, 0xffff0000, v106\n"
    "v_and_b32 v95, 0xffff0000, v107\n"
    "v_mul_f32 v92, s35, v92\n"
    "v_mul_f32 v94, s35, v94\n"
    "v_mul_f32 v93, s35, v93\n"
    "v_mul_f32 v95, s35, v95\n"
    "v_cvt_pk_bf16_f32 v92, v92, v93\n"
    "v_cvt_pk_bf16_f32 v94, v94, v95\n"
    "v_accvgpr_write_b32 a42, v92\n"
    "v_accvgpr_write_b32 a43, v94\n"
    "v_lshlrev_b32 v92, 16, v108\n"
    "v_lshlrev_b32 v94, 16, v109\n"
    "v_and_b32 v93, 0xffff0000, v108\n"
    "v_and_b32 v95, 0xffff0000, v109\n"
    "v_mul_f32 v92, s35, v92\n"
    "v_mul_f32 v94, s35, v94\n"
    "v_mul_f32 v93, s35, v93\n"
    "v_mul_f32 v95, s35, v95\n"
    "v_cvt_pk_bf16_f32 v92, v92, v93\n"
    "v_cvt_pk_bf16_f32 v94, v94, v95\n"
    "v_accvgpr_write_b32 a44, v92\n"
    "v_accvgpr_write_b32 a45, v94\n"
    "v_lshlrev_b32 v92, 16, v110\n"
    "v_lshlrev_b32 v94, 16, v111\n"
    "v_and_b32 v93, 0xffff0000, v110\n"
    "v_and_b32 v95, 0xffff0000, v111\n"
    "v_mul_f32 v92, s35, v92\n"
    "v_mul_f32 v94, s35, v94\n"
    "v_mul_f32 v93, s35, v93\n"
    "v_mul_f32 v95, s35, v95\n"
    "v_cvt_pk_bf16_f32 v92, v92, v93\n"
    "v_cvt_pk_bf16_f32 v94, v94, v95\n"
    "v_accvgpr_write_b32 a46, v92\n"
    "v_accvgpr_write_b32 a47, v94\n"
    "s_waitcnt vmcnt(8) lgkmcnt(0)\n"
    "s_barrier\n"
    "ds_read_b128 a[128:131], v236 offset:1024\n"
    "ds_read_b128 a[132:135], v236 offset:1536\n"
    "ds_read_b128 a[136:139], v236 offset:9216\n"
    "ds_read_b128 a[140:143], v236 offset:9728\n"
    "ds_read_b128 a[144:147], v236 offset:17408\n"
    "ds_read_b128 a[148:151], v236 offset:17920\n"
    "s_add_u32 m0, s61, 0xb400\n"
    "s_nop 0\n"
    "buffer_load_dwordx4 v195, s[16:19], 0 offen offset:3072 nt lds\n"
    "s_waitcnt lgkmcnt(5)\n"
    "v_mfma_f32_16x16x32_bf16 v[128:131], a[128:131], a[8:11], v[128:131]\n"
    "ds_read_b128 a[152:155], v236 offset:25600\n"
    "s_nop 1\n"
    "s_waitcnt lgkmcnt(5)\n"
    "v_mfma_f32_16x16x32_bf16 v[128:131], a[132:135], a[12:15], v[128:131]\n"
    "s_nop 1\n"
    "v_mfma_f32_16x16x32_bf16 v[132:135], a[128:131], a[40:43], v[132:135]\n"
    "ds_read_b128 a[156:159], v236 offset:26112\n"
    "s_nop 1\n"
    "v_mfma_f32_16x16x32_bf16 v[132:135], a[132:135], a[44:47], v[132:135]\n"
    "s_nop 1\n"
    "s_waitcnt lgkmcnt(5)\n"
    "v_mfma_f32_16x16x32_bf16 v[136:139], a[136:139], a[8:11], v[136:139]\n"
    "ds_read_b128 a[160:163], v236 offset:33792\n"
    "s_nop 1\n"
    "s_waitcnt lgkmcnt(5)\n"
    "v_mfma_f32_16x16x32_bf16 v[136:139], a[140:143], a[12:15], v[136:139]\n"
    "s_add_u32 m0, s61, 0xb800\n"
    "s_nop 0\n"
    "buffer_load_dwordx4 v199, s[16:19], s36 offen offset:3072 nt lds\n"
    "s_nop 1\n"
    "v_mfma_f32_16x16x32_bf16 v[140:143], a[136:139], a[40:43], v[140:143]\n"
    "ds_read_b128 a[164:167], v236 offset:34304\n"
    "s_nop 1\n"
    "v_mfma_f32_16x16x32_bf16 v[140:143], a[140:143], a[44:47], v[140:143]\n"
    "PAD0_%=:\ns_cbranch_execz PAD4_%=\n"
    "s_nop 1\n"
    "s_waitcnt lgkmcnt(3)\n"
    "v_mfma_f32_16x16x32_bf16 v[144:147], a[144:147], a[8:11], v[144:147]\n"
    "ds_read_b128 a[168:171], v236 offset:41984\n"
    "s_nop 1\n"
    "v_mfma_f32_16x16x32_bf16 v[144:147], a[148:151], a[12:15], v[144:147]\n"
    "s_nop 1\n"
    "v_mfma_f32_16x16x32_bf16 v[148:151], a[144:147], a[40:43], v[148:151]\n"
    "ds_read_b128 a[172:175], v236 offset:42496\n"
    "s_nop 1\n"
    "v_mfma_f32_16x16x32_bf16 v[148:151], a[148:151], a[44:47], v[148:151]\n"
    "s_nop 1\n"
    "v_mfma_f32_16x16x32_bf16 v[152:155], a[152:155], a[8:11], v[152:155]\n"
    "ds_read_b128 a[176:179], v236 offset:50176\n"
    "s_nop 1\n"
    "s_waitcnt lgkmcnt(4)\n"
    "v_mfma_f32_16x16x32_bf16 v[152:155], a[156:159], a[12:15], v[152:155]\n"
    "s_add_u32 m0, s61, 0xbc00\n"
    "s_nop 0\n"
    "buffer_load_dwordx4 v195, s[16:19], s37 offen offset:3072 nt lds\n"
    "s_nop 1\n"
    "v_mfma_f32_16x16x32_bf16 v[156:159], a[152:155], a[40:43], v[156:159]\n"
    "ds_read_b128 a[180:183], v236 offset:50688\n"
    "s_nop 1\n"
    "v_mfma_f32_16x16x32_bf16 v[156:159], a[156:159], a[44:47], v[156:159]\n"
    "s_nop 1\n"
    "v_mfma_f32_16x16x32_bf16 v[160:163], a[160:163], a[8:11], v[160:163]\n"
    "ds_read_b128 a[184:187], v236 offset:58368\n"
    "s_nop 1\n"
    "s_waitcnt lgkmcnt(3)\n"
    "v_mfma_f32_16x16x32_bf16 v[160:163], a[164:167], a[12:15], v[160:163]\n"
    "s_nop 1\n"
    "v_mfma_f32_16x16x32_bf16 v[164:167], a[160:163], a[40:43], v[164:167]\n"
    "ds_read_b128 a[188:191], v236 offset:58880\n"
    "s_nop 1\n"
    "v_mfma_f32_16x16x32_bf16 v[164:167], a[164:167], a[44:47], v[164:167]\n"
    "s_nop 1\n"
    "v_mfma_f32_16x16x32_bf16 v[168:171], a[168:171], a[8:11], v[168:171]\n"
    "s_nop 1\n"
    "v_mfma_f32_16x16x32_bf16 v[168:171], a[172:175], a[12:15], v[168:171]\n"
    "s_add_u32 m0, s61, 0xc000\n"
    "s_nop 0\n"
    "buffer_load_dwordx4 v199, s[16:19], s60 offen offset:3072 nt lds\n"
    "s_nop 1\n"
    "v_mfma_f32_16x16x32_bf16 v[172:175], a[168:171], a[40:43], v[172:175]\n"
    "s_nop 1\n"
    "v_mfma_f32_16x16x32_bf16 v[172:175], a[172:175], a[44:47], v[172:175]\n"
    "s_nop 1\n"
    "s_waitcnt lgkmcnt(1)\n"
    "v_mfma_f32_16x16x32_bf16 v[176:179], a[176:179], a[8:11], v[176:179]\n"
    "s_nop 1\n"
    "v_mfma_f32_16x16x32_bf16 v[176:179], a[180:183], a[12:15], v[176:179]\n"
    "s_nop 1\n"
    "v_mfma_f32_16x16x32_bf16 v[180:183], a[176:179], a[40:43], v[180:183]\n"
    "s_nop 1\n"
    "v_mfma_f32_16x16x32_bf16 v[180:183], a[180:183], a[44:47], v[180:183]\n"
    "s_nop 1\n"
    "v_mfma_f32_16x16x32_bf16 v[184:187], a[184:187], a[8:11], v[184:187]\n"
    "s_nop 1\n"
    "s_waitcnt lgkmcnt(0)\n"
    "v_mfma_f32_16x16x32_bf16 v[184:187], a[188:191], a[12:15], v[184:187]\n"
    "s_add_u32 m0, s33, 0x0\n"
    "s_nop 0\n"
    "buffer_load_dwordx4 v227, s[20:23], s64 offen offset:3072 nt lds\n"
    "s_nop 1\n"
    "v_mfma_f32_16x16x32_bf16 v[188:191], a[184:187], a[40:43], v[188:191]\n"
    "s_nop 1\n"
    "v_mfma_f32_16x16x32_bf16 v[188:191], a[188:191], a[44:47], v[188:191]\n"
    "s_nop 1\n"
    "s_add_u32 m0, s33, 0x4000\n"
    "s_nop 0\n"
    "buffer_load_dwordx4 v227, s[20:23], s65 offen offset:3072 nt lds\n"
    "v_mfma_f32_32x32x16_bf16 v[64:79], v[8:11], v[4:7], v[64:79]\n"
    "s_add_u32 m0, s33, 0x8000\n"
    "s_nop 0\n"
    "buffer_load_dwordx4 v227, s[20:23], s66 offen offset:3072 nt lds\n"
    "v_mfma_f32_32x32x16_bf16 v[64:79], v[4:7], v[12:15], v[64:79]\n"
    "s_add_u32 m0, s33, 0xc000\n"
    "s_nop 0\n"
    "buffer_load_dwordx4 v227, s[20:23], s67 offen offset:3072 nt lds\n"
    "v_mfma_f32_32x32x16_bf16 v[64:79], v[0:3], v[4:7], v[64:79]\n"
    "s_waitcnt vmcnt(12)\n"
    "ds_read_b128 v[96:99], v235 offset:32768\n"
    "ds_read_b128 v[100:103], v235 offset:33280\n"
    "ds_read_b128 v[104:107], v235 offset:34816\n"
    "ds_read_b128 v[108:111], v235 offset:35328\n"
    "s_waitcnt lgkmcnt(3)\n"
    "v_lshlrev_b32 v92, 16, v96\n"
    "v_lshlrev_b32 v94, 16, v97\n"
    "v_and_b32 v93, 0xffff0000, v96\n"
    "v_and_b32 v95, 0xffff0000, v97\n"
    "v_mul_f32 v92, s35, v92\n"
    "v_mul_f32 v94, s35, v94\n"
    "v_mul_f32 v93, s35, v93\n"
    "v_mul_f32 v95, s35, v95\n"
    "v_cvt_pk_bf16_f32 v92, v92, v93\n"
    "v_cvt_pk_bf16_f32 v94, v94, v95\n"
    "v_accvgpr_write_b32 a16, v92\n"
    "v_accvgpr_write_b32 a17, v94\n"
    "v_lshlrev_b32 v92, 16, v98\n"
    "v_lshlrev_b32 v94, 16, v99\n"
    "v_and_b32 v93, 0xffff0000, v98\n"
    "v_and_b32 v95, 0xffff0000, v99\n"
    "v_mul_f32 v92, s35, v92\n"
    "v_mul_f32 v94, s35, v94\n"
    "v_mul_f32 v93, s35, v93\n"
    "v_mul_f32 v95, s35, v95\n"
    "v_cvt_pk_bf16_f32 v92, v92, v93\n"
    "v_cvt_pk_bf16_f32 v94, v94, v95\n"
    "v_accvgpr_write_b32 a18, v92\n"
    "v_accvgpr_write_b32 a19, v94\n"
    "s_waitcnt lgkmcnt(2)\n"
    "v_lshlrev_b32 v92, 16, v100\n"
    "v_lshlrev_b32 v94, 16, v101\n"
    "v_and_b32 v93, 0xffff0000, v100\n"
    "v_and_b32 v95, 0xffff0000, v101\n"
    "v_mul_f32 v92, s35, v92\n"
    "v_mul_f32 v94, s35, v94\n"
    "v_mul_f32 v93, s35, v93\n"
    "v_mul_f32 v95, s35, v95\n"
    "v_cvt_pk_bf16_f32 v92, v92, v93\n"
    "v_cvt_pk_bf16_f32 v94, v94, v95\n"
    "v_accvgpr_write_b32 a20, v92\n"
    "v_accvgpr_write_b32 a21, v94\n"
    "v_lshlrev_b32 v92, 16, v102\n"
    "v_lshlrev_b32 v94, 16, v103\n"
    "v_and_b32 v93, 0xffff0000, v102\n"
    "v_and_b32 v95, 0xffff0000, v103\n"
    "v_mul_f32 v92, s35, v92\n"
    "v_mul_f32 v94, s35, v94\n"
    "v_mul_f32 v93, s35, v93\n"
    "v_mul_f32 v95, s35, v95\n"
    "v_cvt_pk_bf16_f32 v92, v92, v93\n"
    "v_cvt_pk_bf16_f32 v94, v94, v95\n"
    "v_accvgpr_write_b32 a22, v92\n"
    "v_accvgpr_write_b32 a23, v94\n"
    "s_waitcnt lgkmcnt(0)\n"
    "v_lshlrev_b32 v92, 16, v104\n"
    "v_lshlrev_b32 v94, 16, v105\n"
    "v_and_b32 v93, 0xffff0000, v104\n"
    "v_and_b32 v95, 0xffff0000, v105\n"
    "v_mul_f32 v92, s35, v92\n"
    "v_mul_f32 v94, s35, v94\n"
    "v_mul_f32 v93, s35, v93\n"
    "v_mul_f32 v95, s35, v95\n"
    "v_cvt_pk_bf16_f32 v92, v92, v93\n"
    "v_cvt_pk_bf16_f32 v94, v94, v95\n"
    "v_accvgpr_write_b32 a48, v92\n"
    "v_accvgpr_write_b32 a49, v94\n"
    "v_lshlrev_b32 v92, 16, v106\n"
    "v_lshlrev_b32 v94, 16, v107\n"
    "v_and_b32 v93, 0xffff0000, v106\n"
    "v_and_b32 v95, 0xffff0000, v107\n"
    "v_mul_f32 v92, s35, v92\n"
    "v_mul_f32 v94, s35, v94\n"
    "v_mul_f32 v93, s35, v93\n"
    "v_mul_f32 v95, s35, v95\n"
    "v_cvt_pk_bf16_f32 v92, v92, v93\n"
    "v_cvt_pk_bf16_f32 v94, v94, v95\n"
    "v_accvgpr_write_b32 a50, v92\n"
    "v_accvgpr_write_b32 a51, v94\n"
    "v_lshlrev_b32 v92, 16, v108\n"
    "v_lshlrev_b32 v94, 16, v109\n"
    "v_and_b32 v93, 0xffff0000, v108\n"
    "v_and_b32 v95, 0xffff0000, v109\n"
    "v_mul_f32 v92, s35, v92\n"
    "v_mul_f32 v94, s35, v94\n"
    "v_mul_f32 v93, s35, v93\n"
    "v_mul_f32 v95, s35, v95\n"
    "v_cvt_pk_bf16_f32 v92, v92, v93\n"
    "v_cvt_pk_bf16_f32 v94, v94, v95\n"
    "v_accvgpr_write_b32 a52, v92\n"
    "v_accvgpr_write_b32 a53, v94\n"
    "v_lshlrev_b32 v92, 16, v110\n"
    "v_lshlrev_b32 v94, 16, v111\n"
    "v_and_b32 v93, 0xffff0000, v110\n"
    "v_and_b32 v95, 0xffff0000, v111\n"
    "v_mul_f32 v92, s35, v92\n"
    "v_mul_f32 v94, s35, v94\n"
    "v_mul_f32 v93, s35, v93\n"
    "v_mul_f32 v95, s35, v95\n"
    "v_cvt_pk_bf16_f32 v92, v92, v93\n"
    "v_cvt_pk_bf16_f32 v94, v94, v95\n"
    "v_accvgpr_write_b32 a54, v92\n"
    "v_accvgpr_write_b32 a55, v94\n"
    "s_waitcnt vmcnt(8) lgkmcnt(0)\n"
    "s_barrier\n"
    "ds_read_b128 a[64:67], v236 offset:2048\n"
    "ds_read_b128 a[68:71], v236 offset:2560\n"
    "ds_read_b128 a[72:75], v236 offset:10240\n"
    "ds_read_b128 a[76:79], v236 offset:10752\n"
    "ds_read_b128 a[80:83], v236 offset:18432\n"
    "ds_read_b128 a[84:87], v236 offset:18944\n"
    "s_add_u32 m0, s33, 0x10000\n"
    "s_nop 0\n"
    "buffer_load_dwordx4 v224, s[20:23], s68 offen offset:0 nt lds\n"
    "s_waitcnt lgkmcnt(5)\n"
    "v_mfma_f32_16x16x32_bf16 v[128:131], a[64:67], a[16:19], v[128:131]\n"
    "ds_read_b128 a[88:91], v236 offset:26624\n"
    "s_nop 0\n"
    "s_waitcnt lgkmcnt(5)\n"
    "v_mfma_f32_16x16x32_bf16 v[128:131], a[68:71], a[20:23], v[128:131]\n"
    "s_nop 0\n"
    "v_mfma_f32_16x16x32_bf16 v[132:135], a[64:67], a[48:51], v[132:135]\n"
    "ds_read_b128 a[92:95], v236 offset:27136\n"
    "s_nop 0\n"
    "v_mfma_f32_16x16x32_bf16 v[132:135], a[68:71], a[52:55], v[132:135]\n"
    "s_nop 0\n"
    "s_waitcnt lgkmcnt(5)\n"
    "v_mfma_f32_16x16x32_bf16 v[136:139], a[72:75], a[16:19], v[136:139]\n"
    "ds_read_b128 a[96:99], v236 offset:34816\n"
    "s_nop 0\n"
    "s_waitcnt lgkmcnt(5)\n"
    "v_mfma_f32_16x16x32_bf16 v[136:139], a[76:79], a[20:23], v[136:139]\n"
    "buffer_load_dwordx4 v225, s[20:23], s68 offen offset:1024 nt lds\n"
    "s_nop 0\n"
    "v_mfma_f32_16x16x32_bf16 v[140:143], a[72:75], a[48:51], v[140:143]\n"
    "ds_read_b128 a[100:103], v236 offset:35328\n"
    "s_nop 0\n"
    "v_mfma_f32_16x16x32_bf16 v[140:143], a[76:79], a[52:55], v[140:143]\n"
    "s_nop 0\n"
    "s_waitcnt lgkmcnt(4)\n"
    "v_mfma_f32_16x16x32_bf16 v[144:147], a[80:83], a[16:19], v[144:147]\n"
    "ds_read_b128 a[104:107], v236 offset:43008\n"
    "s_nop 0\n"
    "v_mfma_f32_16x16x32_bf16 v[144:147], a[84:87], a[20:23], v[144:147]\n"
    "s_nop 0\n"
    "v_mfma_f32_16x16x32_bf16 v[148:151], a[80:83], a[48:51], v[148:151]\n"
    "ds_read_b128 a[108:111], v236 offset:43520\n"
    "s_nop 0\n"
    "v_mfma_f32_16x16x32_bf16 v[148:151], a[84:87], a[52:55], v[148:151]\n"
    "s_nop 0\n"
    "s_waitcnt lgkmcnt(4)\n"
    "v_mfma_f32_16x16x32_bf16 v[152:155], a[88:91], a[16:19], v[152:155]\n"
    "ds_read_b128 a[112:115], v236 offset:51200\n"
    "s_nop 0\n"
    "v_mfma_f32_16x16x32_bf16 v[152:155], a[92:95], a[20:23], v[152:155]\n"
    "buffer_load_dwordx4 v226, s[20:23], s68 offen offset:2048 nt lds\n"
    "s_nop 0\n"
    "v_mfma_f32_16x16x32_bf16 v[156:159], a[88:91], a[48:51], v[156:159]\n"
    "ds_read_b128 a[116:119], v236 offset:51712\n"
    "s_nop 0\n"
    "v_mfma_f32_16x16x32_bf16 v[156:159], a[92:95], a[52:55], v[156:159]\n"
    "s_nop 0\n"
    "s_waitcnt lgkmcnt(4)\n"
    "v_mfma_f32_16x16x32_bf16 v[160:163], a[96:99], a[16:19], v[160:163]\n"
    "ds_read_b128 a[120:123], v236 offset:59392\n"
    "s_nop 0\n"
    "v_mfma_f32_16x16x32_bf16 v[160:163], a[100:103], a[20:23], v[160:163]\n"
    "s_nop 0\n"
    "v_mfma_f32_16x16x32_bf16 v[164:167], a[96:99], a[48:51], v[164:167]\n"
    "ds_read_b128 a[124:127], v236 offset:59904\n"
    "s_nop 0\n"
    "v_mfma_f32_16x16x32_bf16 v[164:167], a[100:103], a[52:55], v[164:167]\n"
    "s_nop 0\n"
    "s_waitcnt lgkmcnt(4)\n"
    "v_mfma_f32_16x16x32_bf16 v[168:171], a[104:107], a[16:19], v[168:171]\n"
    "s_nop 0\n"
    "v_mfma_f32_16x16x32_bf16 v[168:171], a[108:111], a[20:23], v[168:171]\n"
    "buffer_load_dwordx4 v227, s[20:23], s68 offen offset:3072 nt lds\n"
    "s_nop 0\n"
    "v_mfma_f32_16x16x32_bf16 v[172:175], a[104:107], a[48:51], v[172:175]\n"
    "s_nop 0\n"
    "v_mfma_f32_16x16x32_bf16 v[172:175], a[108:111], a[52:55], v[172:175]\n"
    "s_nop 0\n"
    "s_waitcnt lgkmcnt(3)\n"
    "v_mfma_f32_16x16x32_bf16 v[176:179], a[112:115], a[16:19], v[176:179]\n"
    "s_nop 0\n"
    "s_waitcnt lgkmcnt(2)\n"
    "v_mfma_f32_16x16x32_bf16 v[176:179], a[116:119], a[20:23], v[176:179]\n"
    "s_nop 0\n"
    "v_mfma_f32_16x16x32_bf16 v[180:183], a[112:115], a[48:51], v[180:183]\n"
    "s_nop 0\n"
    "v_mfma_f32_16x16x32_bf16 v[180:183], a[116:119], a[52:55], v[180:183]\n"
    "s_nop 0\n"
    "s_waitcnt lgkmcnt(1)\n"
    "v_mfma_f32_16x16x32_bf16 v[184:187], a[120:123], a[16:19], v[184:187]\n"
    "s_nop 0\n"
    "s_waitcnt lgkmcnt(0)\n"
    "v_mfma_f32_16x16x32_bf16 v[184:187], a[124:127], a[20:23], v[184:187]\n"
    "s_add_u32 m0, s33, 0x14000\n"
    "s_nop 0\n"
    "buffer_load_dwordx4 v224, s[20:23], s69 offen offset:0 nt lds\n"
    "s_nop 0\n"
    "v_mfma_f32_16x16x32_bf16 v[188:191], a[120:123], a[48:51], v[188:191]\n"
    "s_nop 0\n"
    "v_mfma_f32_16x16x32_bf16 v[188:191], a[124:127], a[52:55], v[188:191]\n"
    "s_nop 0\n"
    "buffer_load_dwordx4 v225, s[20:23], s69 offen offset:1024 nt lds\n"
    "v_mfma_f32_32x32x16_bf16 v[64:79], v[8:11], v[4:7], v[64:79]\n"
    "buffer_load_dwordx4 v226, s[20:23], s69 offen offset:2048 nt lds\n"
    "v_mfma_f32_32x32x16_bf16 v[64:79], v[4:7], v[12:15], v[64:79]\n"
    "buffer_load_dwordx4 v227, s[20:23], s69 offen offset:3072 nt lds\n"
    "v_mfma_f32_32x32x16_bf16 v[64:79], v[0:3], v[4:7], v[64:79]\n"
    "s_waitcnt vmcnt(12)\n"
    "ds_read_b128 v[96:99], v235 offset:49152\n"
    "ds_read_b128 v[100:103], v235 offset:49664\n"
    "ds_read_b128 v[104:107], v235 offset:51200\n"
    "ds_read_b128 v[108:111], v235 offset:51712\n"
    "s_waitcnt lgkmcnt(3)\n"
    "v_lshlrev_b32 v92, 16, v96\n"
    "v_lshlrev_b32 v94, 16, v97\n"
    "v_and_b32 v93, 0xffff0000, v96\n"
    "v_and_b32 v95, 0xffff0000, v97\n"
    "v_mul_f32 v92, s35, v92\n"
    "v_mul_f32 v94, s35, v94\n"
    "v_mul_f32 v93, s35, v93\n"
    "v_mul_f32 v95, s35, v95\n"
    "v_cvt_pk_bf16_f32 v92, v92, v93\n"
    "v_cvt_pk_bf16_f32 v94, v94, v95\n"
    "v_accvgpr_write_b32 a24, v92\n"
    "v_accvgpr_write_b32 a25, v94\n"
    "v_lshlrev_b32 v92, 16, v98\n"
    "v_lshlrev_b32 v94, 16, v99\n"
    "v_and_b32 v93, 0xffff0000, v98\n"
    "v_and_b32 v95, 0xffff0000, v99\n"
    "v_mul_f32 v92, s35, v92\n"
    "v_mul_f32 v94, s35, v94\n"
    "v_mul_f32 v93, s35, v93\n"
    "v_mul_f32 v95, s35, v95\n"
    "v_cvt_pk_bf16_f32 v92, v92, v93\n"
    "v_cvt_pk_bf16_f32 v94, v94, v95\n"
    "v_accvgpr_write_b32 a26, v92\n"
    "v_accvgpr_write_b32 a27, v94\n"
    "s_waitcnt lgkmcnt(2)\n"
    "v_lshlrev_b32 v92, 16, v100\n"
    "v_lshlrev_b32 v94, 16, v101\n"
    "v_and_b32 v93, 0xffff0000, v100\n"
    "v_and_b32 v95, 0xffff0000, v101\n"
    "v_mul_f32 v92, s35, v92\n"
    "v_mul_f32 v94, s35, v94\n"
    "v_mul_f32 v93, s35, v93\n"
    "v_mul_f32 v95, s35, v95\n"
    "v_cvt_pk_bf16_f32 v92, v92, v93\n"
    "v_cvt_pk_bf16_f32 v94, v94, v95\n"
    "v_accvgpr_write_b32 a28, v92\n"
    "v_accvgpr_write_b32 a29, v94\n"
    "v_lshlrev_b32 v92, 16, v102\n"
    "v_lshlrev_b32 v94, 16, v103\n"
    "v_and_b32 v93, 0xffff0000, v102\n"
    "v_and_b32 v95, 0xffff0000, v103\n"
    "v_mul_f32 v92, s35, v92\n"
    "v_mul_f32 v94, s35, v94\n"
    "v_mul_f32 v93, s35, v93\n"
    "v_mul_f32 v95, s35, v95\n"
    "v_cvt_pk_bf16_f32 v92, v92, v93\n"
    "v_cvt_pk_bf16_f32 v94, v94, v95\n"
    "v_accvgpr_write_b32 a30, v92\n"
    "v_accvgpr_write_b32 a31, v94\n"
    "s_waitcnt lgkmcnt(0)\n"
    "v_lshlrev_b32 v92, 16, v104\n"
    "v_lshlrev_b32 v94, 16, v105\n"
    "v_and_b32 v93, 0xffff0000, v104\n"
    "v_and_b32 v95, 0xffff0000, v105\n"
    "v_mul_f32 v92, s35, v92\n"
    "v_mul_f32 v94, s35, v94\n"
    "v_mul_f32 v93, s35, v93\n"
    "v_mul_f32 v95, s35, v95\n"
    "v_cvt_pk_bf16_f32 v92, v92, v93\n"
    "v_cvt_pk_bf16_f32 v94, v94, v95\n"
    "v_accvgpr_write_b32 a56, v92\n"
    "v_accvgpr_write_b32 a57, v94\n"
    "v_lshlrev_b32 v92, 16, v106\n"
    "v_lshlrev_b32 v94, 16, v107\n"
    "v_and_b32 v93, 0xffff0000, v106\n"
    "v_and_b32 v95, 0xffff0000, v107\n"
    "v_mul_f32 v92, s35, v92\n"
    "v_mul_f32 v94, s35, v94\n"
    "v_mul_f32 v93, s35, v93\n"
    "v_mul_f32 v95, s35, v95\n"
    "v_cvt_pk_bf16_f32 v92, v92, v93\n"
    "v_cvt_pk_bf16_f32 v94, v94, v95\n"
    "v_accvgpr_write_b32 a58, v92\n"
    "v_accvgpr_write_b32 a59, v94\n"
    "v_lshlrev_b32 v92, 16, v108\n"
    "v_lshlrev_b32 v94, 16, v109\n"
    "v_and_b32 v93, 0xffff0000, v108\n"
    "v_and_b32 v95, 0xffff0000, v109\n"
    "v_mul_f32 v92, s35, v92\n"
    "v_mul_f32 v94, s35, v94\n"
    "v_mul_f32 v93, s35, v93\n"
    "v_mul_f32 v95, s35, v95\n"
    "v_cvt_pk_bf16_f32 v92, v92, v93\n"
    "v_cvt_pk_bf16_f32 v94, v94, v95\n"
    "v_accvgpr_write_b32 a60, v92\n"
    "v_accvgpr_write_b32 a61, v94\n"
    "v_lshlrev_b32 v92, 16, v110\n"
    "v_lshlrev_b32 v94, 16, v111\n"
    "v_and_b32 v93, 0xffff0000, v110\n"
    "v_and_b32 v95, 0xffff0000, v111\n"
    "v_mul_f32 v92, s35, v92\n"
    "v_mul_f32 v94, s35, v94\n"
    "v_mul_f32 v93, s35, v93\n"
    "v_mul_f32 v95, s35, v95\n"
    "v_cvt_pk_bf16_f32 v92, v92, v93\n"
    "v_cvt_pk_bf16_f32 v94, v94, v95\n"
    "v_accvgpr_write_b32 a62, v92\n"
    "v_accvgpr_write_b32 a63, v94\n"
    "s_waitcnt vmcnt(8) lgkmcnt(0)\n"
    "s_barrier\n"
    "ds_read_b128 a[128:131], v236 offset:3072\n"
    "ds_read_b128 a[132:135], v236 offset:3584\n"
    "ds_read_b128 a[136:139], v236 offset:11264\n"
    "ds_read_b128 a[140:143], v236 offset:11776\n"
    "ds_read_b128 a[144:147], v236 offset:19456\n"
    "ds_read_b128 a[148:151], v236 offset:19968\n"
    "s_add_u32 m0, s33, 0x18000\n"
    "s_nop 0\n"
    "buffer_load_dwordx4 v224, s[20:23], s70 offen offset:0 nt lds\n"
    "s_waitcnt lgkmcnt(5)\n"
    "v_mfma_f32_16x16x32_bf16 v[128:131], a[128:131], a[24:27], v[128:131]\n"
    "ds_read_b128 a[152:155], v236 offset:27648\n"
    "s_waitcnt lgkmcnt(5)\n"
    "v_mfma_f32_16x16x32_bf16 v[128:131], a[132:135], a[28:31], v[128:131]\n"
    "v_mfma_f32_16x16x32_bf16 v[132:135], a[128:131], a[56:59], v[132:135]\n"
    "ds_read_b128 a[156:159], v236 offset:28160\n"
    "v_mfma_f32_16x16x32_bf16 v[132:135], a[132:135], a[60:63], v[132:135]\n"
    "s_waitcnt lgkmcnt(5)\n"
    "v_mfma_f32_16x16x32_bf16 v[136:139], a[136:139], a[24:27], v[136:139]\n"
    "ds_read_b128 a[160:163], v236 offset:35840\n"
    "s_waitcnt lgkmcnt(5)\n"
    "v_mfma_f32_16x16x32_bf16 v[136:139], a[140:143], a[28:31], v[136:139]\n"
    "v_mfma_f32_16x16x32_bf16 v[140:143], a[136:139], a[56:59], v[140:143]\n"
    "ds_read_b128 a[164:167], v236 offset:36352\n"
    "v_mfma_f32_16x16x32_bf16 v[140:143], a[140:143], a[60:63], v[140:143]\n"
    "s_waitcnt lgkmcnt(5)\n"
    "v_mfma_f32_16x16x32_bf16 v[144:147], a[144:147], a[24:27], v[144:147]\n"
    "ds_read_b128 a[168:171], v236 offset:44032\n"
    "s_waitcnt lgkmcnt(5)\n"
    "v_mfma_f32_16x16x32_bf16 v[144:147], a[148:151], a[28:31], v[144:147]\n"
    "v_mfma_f32_16x16x32_bf16 v[148:151], a[144:147], a[56:59], v[148:151]\n"
    "ds_read_b128 a[172:175], v236 offset:44544\n"
    "v_mfma_f32_16x16x32_bf16 v[148:151], a[148:151], a[60:63], v[148:151]\n"
    "s_waitcnt lgkmcnt(5)\n"
    "v_mfma_f32_16x16x32_bf16 v[152:155], a[152:155], a[24:27], v[152:155]\n"
    "ds_read_b128 a[176:179], v236 offset:52224\n"
    "s_waitcnt lgkmcnt(5)\n"
    "v_mfma_f32_16x16x32_bf16 v[152:155], a[156:159], a[28:31], v[152:155]\n"
    "v_mfma_f32_16x16x32_bf16 v[156:159], a[152:155], a[56:59], v[156:159]\n"
    "ds_read_b128 a[180:183], v236 offset:52736\n"
    "v_mfma_f32_16x16x32_bf16 v[156:159], a[156:159], a[60:63], v[156:159]\n"
    "s_waitcnt lgkmcnt(5)\n"
    "v_mfma_f32_16x16x32_bf16 v[160:163], a[160:163], a[24:27], v[160:163]\n"
    "ds_read_b128 a[184:187], v236 offset:60416\n"
    "s_waitcnt lgkmcnt(5)\n"
    "v_mfma_f32_16x16x32_bf16 v[160:163], a[164:167], a[28:31], v[160:163]\n"
    "v_mfma_f32_16x16x32_bf16 v[164:167], a[160:163], a[56:59], v[164:167]\n"
    "ds_read_b128 a[188:191], v236 offset:60928\n"
    "v_mfma_f32_16x16x32_bf16 v[164:167], a[164:167], a[60:63], v[164:167]\n"
    "s_waitcnt lgkmcnt(5)\n"
    "v_mfma_f32_16x16x32_bf16 v[168:171], a[168:171], a[24:27], v[168:171]\n"
    "s_waitcnt lgkmcnt(4)\n"
    "v_mfma_f32_16x16x32_bf16 v[168:171], a[172:175], a[28:31], v[168:171]\n"
    "v_mfma_f32_16x16x32_bf16 v[172:175], a[168:171], a[56:59], v[172:175]\n"
    "v_mfma_f32_16x16x32_bf16 v[172:175], a[172:175], a[60:63], v[172:175]\n"
    "s_waitcnt lgkmcnt(3)\n"
    "v_mfma_f32_16x16x32_bf16 v[176:179], a[176:179], a[24:27], v[176:179]\n"
    "s_waitcnt lgkmcnt(2)\n"
    "v_mfma_f32_16x16x32_bf16 v[176:179], a[180:183], a[28:31], v[176:179]\n"
    "v_mfma_f32_16x16x32_bf16 v[180:183], a[176:179], a[56:59], v[180:183]\n"
    "v_mfma_f32_16x16x32_bf16 v[180:183], a[180:183], a[60:63], v[180:183]\n"
    "s_waitcnt lgkmcnt(1)\n"
    "v_mfma_f32_16x16x32_bf16 v[184:187], a[184:187], a[24:27], v[184:187]\n"
    "s_waitcnt lgkmcnt(0)\n"
    "v_mfma_f32_16x16x32_bf16 v[184:187], a[188:191], a[28:31], v[184:187]\n"
    "v_mfma_f32_16x16x32_bf16 v[188:191], a[184:187], a[56:59], v[188:191]\n"
    "v_mfma_f32_16x16x32_bf16 v[188:191], a[188:191], a[60:63], v[188:191]\n"
    "s_waitcnt vmcnt(5) lgkmcnt(0)\n"
    "s_barrier\n"
    "ds_read_b128 a[64:67], v237 offset:0\n"
    "ds_read_b128 a[68:71], v237 offset:512\n"
    "ds_read_b128 a[72:75], v237 offset:1024\n"
    "ds_read_b128 a[76:79], v237 offset:1536\n"
    "ds_read_b128 a[80:83], v237 offset:2048\n"
    "ds_read_b128 a[84:87], v237 offset:2560\n"
    "ds_read_b128 a[88:91], v237 offset:3072\n"
    "ds_read_b128 a[92:95], v237 offset:3584\n"
    "buffer_load_dwordx4 v225, s[20:23], s70 offen offset:1024 nt lds\n"
    "buffer_load_dwordx4 v226, s[20:23], s70 offen offset:2048 nt lds\n"
    "buffer_load_dwordx4 v227, s[20:23], s70 offen offset:3072 nt lds\n"
    "ds_read_b128 a[96:99], v237 offset:8192\n"
    "ds_read_b128 a[100:103], v237 offset:8704\n"
    "ds_read_b128 a[104:107], v237 offset:9216\n"
    "ds_read_b128 a[108:111], v237 offset:9728\n"
    "v_exp_f32 v128, v128\n"
    "v_exp_f32 v129, v129\n"
    "v_exp_f32 v130, v130\n"
    "v_exp_f32 v131, v131\n"
    "v_cvt_pk_bf16_f32 v0, v128, v129\n"
    "v_exp_f32 v136, v136\n"
    "ds_read_b128 a[112:115], v237 offset:10240\n"
    "ds_read_b128 a[116:119], v237 offset:10752\n"
    "ds_read_b128 a[120:123], v237 offset:11264\n"
    "ds_read_b128 a[124:127], v237 offset:11776\n"
    "v_exp_f32 v137, v137\n"
    "v_cvt_pk_bf16_f32 v1, v130, v131\n"
    "v_exp_f32 v138, v138\n"
    "v_exp_f32 v139, v139\n"
    "s_waitcnt lgkmcnt(11)\n"
    "v_mfma_f32_16x16x32_bf16 v[112:115], a[64:67], a[0:3], 0\n"
    "v_cvt_pk_bf16_f32 v2, v136, v137\n"
    "v_exp_f32 v132, v132\n"
    "v_mfma_f32_16x16x32_bf16 v[112:115], a[68:71], a[4:7], v[112:115]\n"
    "s_add_u32 m0, s33, 0x1c000\n"
    "v_exp_f32 v133, v133\n"
    "v_mfma_f32_16x16x32_bf16 v[112:115], a[72:75], a[8:11], v[112:115]\n"
    "buffer_load_dwordx4 v224, s[20:23], s71 offen offset:0 nt lds\n"
    "v_cvt_pk_bf16_f32 v3, v138, v139\n"
    "v_mfma_f32_16x16x32_bf16 v[112:115], a[76:79], a[12:15], v[112:115]\n"
    "v_exp_f32 v134, v134\n"
    "v_mfma_f32_16x16x32_bf16 v[112:115], a[80:83], a[16:19], v[112:115]\n"
    "buffer_load_dwordx4 v225, s[20:23], s71 offen offset:1024 nt lds\n"
    "s_waitcnt lgkmcnt(4)\n"
    "v_mfma_f32_16x16x32_bf16 v[112:115], a[84:87], a[20:23], v[112:115]\n"
    "v_exp_f32 v135, v135\n"
    "v_mfma_f32_16x16x32_bf16 v[112:115], a[88:91], a[24:27], v[112:115]\n"
    "buffer_load_dwordx4 v226, s[20:23], s71 offen offset:2048 nt lds\n"
    "v_cvt_pk_bf16_f32 v4, v132, v133\n"
    "v_mfma_f32_16x16x32_bf16 v[112:115], a[92:95], a[28:31], v[112:115]\n"
    "v_exp_f32 v140, v140\n"
    "v_mfma_f32_16x16x32_bf16 v[116:119], a[64:67], a[32:35], 0\n"
    "v_exp_f32 v141, v141\n"
    "v_mfma_f32_16x16x32_bf16 v[116:119], a[68:71], a[36:39], v[116:119]\n"
    "s_waitcnt vmcnt(7) lgkmcnt(0)\n"
    "s_barrier\n"
    "v_mfma_f32_16x16x32_bf16 v[116:119], a[72:75], a[40:43], v[116:119]\n"
    "buffer_load_dwordx4 v227, s[20:23], s71 offen offset:3072 nt lds\n"
    "v_mfma_f32_16x16x32_bf16 v[116:119], a[76:79], a[44:47], v[116:119]\n"
    "ds_read_b128 a[128:131], v237 offset:16384\n"
    "s_add_u32 m0, s33, 0x20000\n"
    "v_mfma_f32_16x16x32_bf16 v[116:119], a[80:83], a[48:51], v[116:119]\n"
    "buffer_load_dwordx4 v224, s[20:23], s72 offen offset:0 nt lds\n"
    "v_cvt_pk_bf16_f32 v5, v134, v135\n"
    "v_mfma_f32_16x16x32_bf16 v[116:119], a[84:87], a[52:55], v[116:119]\n"
    "ds_read_b128 a[132:135], v237 offset:16896\n"
    "v_exp_f32 v142, v142\n"
    "v_mfma_f32_16x16x32_bf16 v[116:119], a[88:91], a[56:59], v[116:119]\n"
    "buffer_load_dwordx4 v225, s[20:23], s72 offen offset:1024 nt lds\n"
    "v_exp_f32 v143, v143\n"
    "v_mfma_f32_16x16x32_bf16 v[116:119], a[92:95], a[60:63], v[116:119]\n"
    "ds_read_b128 a[136:139], v237 offset:17408\n"
    "v_cvt_pk_bf16_f32 v6, v140, v141\n"
    "v_mfma_f32_16x16x32_bf16 v[120:123], a[96:99], a[0:3], 0\n"
    "buffer_load_dwordx4 v226, s[20:23], s72 offen offset:2048 nt lds\n"
    "v_mfma_f32_16x16x32_bf16 v[120:123], a[100:103], a[4:7], v[120:123]\n"
    "ds_read_b128 a[140:143], v237 offset:17920\n"
    "v_cvt_pk_bf16_f32 v7, v142, v143\n"
    "v_mfma_f32_16x16x32_bf16 v[120:123], a[104:107], a[8:11], v[120:123]\n"
    "buffer_load_dwordx4 v227, s[20:23], s72 offen offset:3072 nt lds\n"
    "v_exp_f32 v144, v144\n"
    "v_mfma_f32_16x16x32_bf16 v[120:123], a[108:111], a[12:15], v[120:123]\n"
    "ds_read_b128 a[144:147], v237 offset:18432\n"
    "v_exp_f32 v145, v145\n"
    "v_mfma_f32_16x16x32_bf16 v[120:123], a[112:115], a[16:19], v[120:123]\n"
    "ds_read_b128 a[148:151], v237 offset:18944\n"
    "v_exp_f32 v146, v146\n"
    "v_mfma_f32_16x16x32_bf16 v[120:123], a[116:119], a[20:23], v[120:123]\n"
    "ds_read_b128 a[152:155], v237 offset:19456\n"
    "v_exp_f32 v147, v147\n"
    "v_mfma_f32_16x16x32_bf16 v[120:123], a[120:123], a[24:27], v[120:123]\n"
    "ds_read_b128 a[156:159], v237 offset:19968\n"
    "v_cvt_pk_bf16_f32 v8, v144, v145\n"
    "v_mfma_f32_16x16x32_bf16 v[120:123], a[124:127], a[28:31], v[120:123]\n"
    "ds_read_b128 a[160:163], v237 offset:24576\n"
    "s_add_u32 m0, s33, 0x24000\n"
    "v_exp_f32 v152, v152\n"
    "v_exp_f32 v153, v153\n"
    "v_mfma_f32_16x16x32_bf16 v[124:127], a[96:99], a[32:35], 0\n"
    "buffer_load_dwordx4 v224, s[20:23], s73 offen offset:0 nt lds\n"
    "v_cvt_pk_bf16_f32 v9, v146, v147\n"
    "v_mfma_f32_16x16x32_bf16 v[124:127], a[100:103], a[36:39], v[124:127]\n"
    "ds_read_b128 a[164:167], v237 offset:25088\n"
    "v_exp_f32 v154, v154\n"
    "v_mfma_f32_16x16x32_bf16 v[204:207], v[200:203], v[0:3], 0\n"
    "ds_read_b128 a[168:171], v237 offset:25600\n"
    "v_exp_f32 v155, v155\n"
    "v_mfma_f32_16x16x32_bf16 v[124:127], a[104:107], a[40:43], v[124:127]\n"
    "ds_read_b128 a[172:175], v237 offset:26112\n"
    "v_cvt_pk_bf16_f32 v10, v152, v153\n"
    "v_mfma_f32_16x16x32_bf16 v[124:127], a[108:111], a[44:47], v[124:127]\n"
    "ds_read_b128 a[176:179], v237 offset:26624\n"
    "v_exp_f32 v148, v148\n"
    "v_mfma_f32_16x16x32_bf16 v[124:127], a[112:115], a[48:51], v[124:127]\n"
    "ds_read_b128 a[180:183], v237 offset:27136\n"
    "v_exp_f32 v149, v149\n"
    "v_mfma_f32_16x16x32_bf16 v[124:127], a[116:119], a[52:55], v[124:127]\n"
    "ds_read_b128 a[184:187], v237 offset:27648\n"
    "v_cvt_pk_bf16_f32 v11, v154, v155\n"
    "v_mfma_f32_16x16x32_bf16 v[124:127], a[120:123], a[56:59], v[124:127]\n"
    "v_exp_f32 v150, v150\n"
    "v_mfma_f32_16x16x32_bf16 v[124:127], a[124:127], a[60:63], v[124:127]\n"
    "ds_read_b128 a[188:191], v237 offset:28160\n"
    "v_exp_f32 v151, v151\n"
    "s_waitcnt vmcnt(9) lgkmcnt(6)\n"
    "s_barrier\n"
    "v_mfma_f32_16x16x32_bf16 v[252:255], v[200:203], v[4:7], 0\n"
    "v_cvt_pk_bf16_f32 v12, v148, v149\n"
    "v_exp_f32 v156, v156\n"
    "v_mfma_f32_16x16x32_bf16 v[128:131], a[128:131], a[0:3], 0\n"
    "v_exp_f32 v157, v157\n"
    "v_mfma_f32_16x16x32_bf16 v[128:131], a[132:135], a[4:7], v[128:131]\n"
    "ds_read_b128 a[64:67], v237 offset:32768\n"
    "v_cvt_pk_bf16_f32 v13, v150, v151\n"
    "v_mfma_f32_16x16x32_bf16 v[128:131], a[136:139], a[8:11], v[128:131]\n"
    "buffer_load_dwordx4 v225, s[20:23], s73 offen offset:1024 nt lds\n"
    "v_exp_f32 v158, v158\n"
    "v_mfma_f32_16x16x32_bf16 v[128:131], a[140:143], a[12:15], v[128:131]\n"
    "ds_read_b128 a[68:71], v237 offset:33280\n"
    "v_exp_f32 v159, v159\n"
    "v_mfma_f32_16x16x32_bf16 v[128:131], a[144:147], a[16:19], v[128:131]\n"
    "buffer_load_dwordx4 v226, s[20:23], s73 offen offset:2048 nt lds\n"
    "v_cvt_pk_bf16_f32 v14, v156, v157\n"
    "v_mfma_f32_16x16x32_bf16 v[128:131], a[148:151], a[20:23], v[128:131]\n"
    "ds_read_b128 a[72:75], v237 offset:33792\n"
    "v_cvt_pk_bf16_f32 v15, v158, v159\n"
    "v_mfma_f32_16x16x32_bf16 v[128:131], a[152:155], a[24:27], v[128:131]\n"
    "v_exp_f32 v160, v160\n"
    "v_mfma_f32_16x16x32_bf16 v[128:131], a[156:159], a[28:31], v[128:131]\n"
    "ds_read_b128 a[76:79], v237 offset:34304\n"
    "v_exp_f32 v161, v161\n"
    "v_mfma_f32_16x16x32_bf16 v[132:135], a[128:131], a[32:35], 0\n"
    "buffer_load_dwordx4 v227, s[20:23], s73 offen offset:3072 nt lds\n"
    "v_exp_f32 v162, v162\n"
    "v_mfma_f32_16x16x32_bf16 v[132:135], a[132:135], a[36:39], v[132:135]\n"
    "ds_read_b128 a[80:83], v237 offset:34816\n"
    "v_exp_f32 v163, v163\n"
    "v_mfma_f32_16x16x32_bf16 v[132:135], a[136:139], a[40:43], v[132:135]\n"
    "v_cvt_pk_bf16_f32 v16, v160, v161\n"
    "v_exp_f32 v168, v168\n"
    "v_mfma_f32_16x16x32_bf16 v[132:135], a[140:143], a[44:47], v[132:135]\n"
    "v_exp_f32 v169, v169\n"
    "v_mfma_f32_16x16x32_bf16 v[132:135], a[144:147], a[48:51], v[132:135]\n"
    "ds_read_b128 a[84:87], v237 offset:35328\n"
    "v_cvt_pk_bf16_f32 v17, v162, v163\n"
    "v_mfma_f32_16x16x32_bf16 v[132:135], a[148:151], a[52:55], v[132:135]\n"
    "ds_read_b128 a[88:91], v237 offset:35840\n"
    "v_exp_f32 v170, v170\n"
    "v_mfma_f32_16x16x32_bf16 v[204:207], v[200:203], v[8:11], v[204:207]\n"
    "s_add_u32 m0, s33, 0x0\n"
    "v_exp_f32 v171, v171\n"
    "PAD1_%=:\ns_cbranch_execz PAD5_%=\n"
    "v_mfma_f32_16x16x32_bf16 v[132:135], a[152:155], a[56:59], v[132:135]\n"
    "buffer_load_dwordx4 v224, s[20:23], s74 offen offset:0 nt lds\n"
    "v_cvt_pk_bf16_f32 v18, v168, v169\n"
    "v_mfma_f32_16x16x32_bf16 v[132:135], a[156:159], a[60:63], v[132:135]\n"
    "ds_read_b128 a[92:95], v237 offset:36352\n"
    "v_exp_f32 v164, v164\n"
    "v_mfma_f32_16x16x32_bf16 v[136:139], a[160:163], a[0:3], 0\n"
    "v_exp_f32 v165, v165\n"
    "v_mfma_f32_16x16x32_bf16 v[136:139], a[164:167], a[4:7], v[136:139]\n"
    "ds_read_b128 a[96:99], v237 offset:40960\n"
    "v_cvt_pk_bf16_f32 v19, v170, v171\n"
    "s_waitcnt lgkmcnt(4)\n"
    "v_mfma_f32_16x16x32_bf16 v[136:139], a[168:171], a[8:11], v[136:139]\n"
    "v_exp_f32 v166, v166\n"
    "v_mfma_f32_16x16x32_bf16 v[136:139], a[172:175], a[12:15], v[136:139]\n"
    "ds_read_b128 a[100:103], v237 offset:41472\n"
    "v_exp_f32 v167, v167\n"
    "v_mfma_f32_16x16x32_bf16 v[252:255], v[200:203], v[12:15], v[252:255]\n"
    "ds_read_b128 a[104:107], v237 offset:41984\n"
    "v_cvt_pk_bf16_f32 v20, v164, v165\n"
    "v_mfma_f32_16x16x32_bf16 v[136:139], a[176:179], a[16:19], v[136:139]\n"
    "buffer_load_dwordx4 v225, s[20:23], s74 offen offset:1024 nt lds\n"
    "v_exp_f32 v172, v172\n"
    "v_mfma_f32_16x16x32_bf16 v[136:139], a[180:183], a[20:23], v[136:139]\n"
    "v_exp_f32 v173, v173\n"
    "v_cvt_pk_bf16_f32 v21, v166, v167\n"
    "v_mfma_f32_16x16x32_bf16 v[136:139], a[184:187], a[24:27], v[136:139]\n"
    "ds_read_b128 a[108:111], v237 offset:42496\n"
    "v_exp_f32 v174, v174\n"
    "v_mfma_f32_16x16x32_bf16 v[136:139], a[188:191], a[28:31], v[136:139]\n"
    "v_exp_f32 v175, v175\n"
    "v_mfma_f32_16x16x32_bf16 v[140:143], a[160:163], a[32:35], 0\n"
    "ds_read_b128 a[112:115], v237 offset:43008\n"
    "v_cvt_pk_bf16_f32 v22, v172, v173\n"
    "v_mfma_f32_16x16x32_bf16 v[140:143], a[164:167], a[36:39], v[140:143]\n"
    "ds_read_b128 a[116:119], v237 offset:43520\n"
    "v_cvt_pk_bf16_f32 v23, v174, v175\n"
    "v_mfma_f32_16x16x32_bf16 v[140:143], a[168:171], a[40:43], v[140:143]\n"
    "buffer_load_dwordx4 v226, s[20:23], s74 offen offset:2048 nt lds\n"
    "v_exp_f32 v176, v176\n"
    "v_mfma_f32_16x16x32_bf16 v[140:143], a[172:175], a[44:47], v[140:143]\n"
    "v_exp_f32 v177, v177\n"
    "v_mfma_f32_16x16x32_bf16 v[140:143], a[176:179], a[48:51], v[140:143]\n"
    "ds_read_b128 a[120:123], v237 offset:44032\n"
    "v_exp_f32 v178, v178\n"
    "v_mfma_f32_16x16x32_bf16 v[140:143], a[180:183], a[52:55], v[140:143]\n"
    "v_exp_f32 v179, v179\n"
    "v_mfma_f32_16x16x32_bf16 v[140:143], a[184:187], a[56:59], v[140:143]\n"
    "ds_read_b128 a[124:127], v237 offset:44544\n"
    "v_cvt_pk_bf16_f32 v24, v176, v177\n"
    "v_mfma_f32_16x16x32_bf16 v[140:143], a[188:191], a[60:63], v[140:143]\n"
    "v_exp_f32 v184, v184\n"
    "s_waitcnt vmcnt(11) lgkmcnt(4)\n"
    "s_barrier\n"
    "v_mfma_f32_16x16x32_bf16 v[144:147], a[64:67], a[0:3], 0\n"
    "v_exp_f32 v185, v185\n"
    "v_mfma_f32_16x16x32_bf16 v[144:147], a[68:71], a[4:7], v[144:147]\n"
    "ds_read_b128 a[128:131], v237 offset:49152\n"
    "v_cvt_pk_bf16_f32 v25, v178, v179\n"
    "v_mfma_f32_16x16x32_bf16 v[204:207], v[200:203], v[16:19], v[204:207]\n"
    "v_exp_f32 v186, v186\n"
    "v_mfma_f32_16x16x32_bf16 v[144:147], a[72:75], a[8:11], v[144:147]\n"
    "buffer_load_dwordx4 v227, s[20:23], s74 offen offset:3072 nt lds\n"
    "v_exp_f32 v187, v187\n"
    "v_mfma_f32_16x16x32_bf16 v[144:147], a[76:79], a[12:15], v[144:147]\n"
    "ds_read_b128 a[132:135], v237 offset:49664\n"
    "v_cvt_pk_bf16_f32 v26, v184, v185\n"
    "v_mfma_f32_16x16x32_bf16 v[144:147], a[80:83], a[16:19], v[144:147]\n"
    "ds_read_b128 a[136:139], v237 offset:50176\n"
    "v_exp_f32 v180, v180\n"
    "v_mfma_f32_16x16x32_bf16 v[144:147], a[84:87], a[20:23], v[144:147]\n"
    "s_add_u32 m0, s33, 0x4000\n"
    "v_exp_f32 v181, v181\n"
    "v_mfma_f32_16x16x32_bf16 v[144:147], a[88:91], a[24:27], v[144:147]\n"
    "buffer_load_dwordx4 v224, s[20:23], s75 offen offset:0 nt lds\n"
    "v_cvt_pk_bf16_f32 v27, v186, v187\n"
    "v_mfma_f32_16x16x32_bf16 v[144:147], a[92:95], a[28:31], v[144:147]\n"
    "ds_read_b128 a[140:143], v237 offset:50688\n"
    "v_exp_f32 v182, v182\n"
    "v_mfma_f32_16x16x32_bf16 v[252:255], v[200:203], v[20:23], v[252:255]\n"
    "v_exp_f32 v183, v183\n"
    "v_mfma_f32_16x16x32_bf16 v[148:151], a[64:67], a[32:35], 0\n"
    "ds_read_b128 a[144:147], v237 offset:51200\n"
    "v_cvt_pk_bf16_f32 v28, v180, v181\n"
    "v_mfma_f32_16x16x32_bf16 v[148:151], a[68:71], a[36:39], v[148:151]\n"
    "v_exp_f32 v188, v188\n"
    "v_mfma_f32_16x16x32_bf16 v[148:151], a[72:75], a[40:43], v[148:151]\n"
    "buffer_load_dwordx4 v225, s[20:23], s75 offen offset:1024 nt lds\n"
    "v_exp_f32 v189, v189\n"
    "v_mfma_f32_16x16x32_bf16 v[148:151], a[76:79], a[44:47], v[148:151]\n"
    "ds_read_b128 a[148:151], v237 offset:51712\n"
    "v_cvt_pk_bf16_f32 v29, v182, v183\n"
    "v_mfma_f32_16x16x32_bf16 v[148:151], a[80:83], a[48:51], v[148:151]\n"
    "ds_read_b128 a[152:155], v237 offset:52224\n"
    "v_exp_f32 v190, v190\n"
    "v_mfma_f32_16x16x32_bf16 v[148:151], a[84:87], a[52:55], v[148:151]\n"
    "v_exp_f32 v191, v191\n"
    "v_mfma_f32_16x16x32_bf16 v[148:151], a[88:91], a[56:59], v[148:151]\n"
    "ds_read_b128 a[156:159], v237 offset:52736\n"
    "v_cvt_pk_bf16_f32 v30, v188, v189\n"
    "v_mfma_f32_16x16x32_bf16 v[148:151], a[92:95], a[60:63], v[148:151]\n"
    "ds_read_b128 a[160:163], v237 offset:57344\n"
    "v_cvt_pk_bf16_f32 v31, v190, v191\n"
    "v_mfma_f32_16x16x32_bf16 v[152:155], a[96:99], a[0:3], 0\n"
    "buffer_load_dwordx4 v226, s[20:23], s75 offen offset:2048 nt lds\n"
    "v_exp_f32 v112, v112\n"
    "v_mfma_f32_16x16x32_bf16 v[152:155], a[100:103], a[4:7], v[152:155]\n"
    "v_exp_f32 v113, v113\n"
    "v_mfma_f32_16x16x32_bf16 v[152:155], a[104:107], a[8:11], v[152:155]\n"
    "v_exp_f32 v114, v114\n"
    "v_mfma_f32_16x16x32_bf16 v[152:155], a[108:111], a[12:15], v[152:155]\n"
    "v_exp_f32 v115, v115\n"
    "s_waitcnt lgkmcnt(3)\n"
    "v_mfma_f32_16x16x32_bf16 v[152:155], a[112:115], a[16:19], v[152:155]\n"
    "ds_read_b128 a[164:167], v237 offset:57856\n"
    "v_cvt_pk_bf16_f32 v32, v112, v113\n"
    "v_mfma_f32_16x16x32_bf16 v[152:155], a[116:119], a[20:23], v[152:155]\n"
    "ds_read_b128 a[168:171], v237 offset:58368\n"
    "v_exp_f32 v120, v120\n"
    "v_mfma_f32_16x16x32_bf16 v[152:155], a[120:123], a[24:27], v[152:155]\n"
    "buffer_load_dwordx4 v227, s[20:23], s75 offen offset:3072 nt lds\n"
    "v_exp_f32 v121, v121\n"
    "v_mfma_f32_16x16x32_bf16 v[152:155], a[124:127], a[28:31], v[152:155]\n"
    "ds_read_b128 a[172:175], v237 offset:58880\n"
    "v_cvt_pk_bf16_f32 v33, v114, v115\n"
    "v_mfma_f32_16x16x32_bf16 v[204:207], v[200:203], v[24:27], v[204:207]\n"
    "v_exp_f32 v122, v122\n"
    "v_mfma_f32_16x16x32_bf16 v[156:159], a[96:99], a[32:35], 0\n"
    "v_exp_f32 v123, v123\n"
    "v_mfma_f32_16x16x32_bf16 v[156:159], a[100:103], a[36:39], v[156:159]\n"
    "ds_read_b128 a[176:179], v237 offset:59392\n"
    "v_cvt_pk_bf16_f32 v34, v120, v121\n"
    "v_mfma_f32_16x16x32_bf16 v[156:159], a[104:107], a[40:43], v[156:159]\n"
    "v_exp_f32 v116, v116\n"
    "v_mfma_f32_16x16x32_bf16 v[156:159], a[108:111], a[44:47], v[156:159]\n"
    "ds_read_b128 a[180:183], v237 offset:59904\n"
    "s_add_u32 m0, s33, 0x8000\n"
    "v_exp_f32 v117, v117\n"
    "v_mfma_f32_16x16x32_bf16 v[156:159], a[112:115], a[48:51], v[156:159]\n"
    "buffer_load_dwordx4 v224, s[20:23], s76 offen offset:0 nt lds\n"
    "v_cvt_pk_bf16_f32 v35, v122, v123\n"
    "v_mfma_f32_16x16x32_bf16 v[156:159], a[116:119], a[52:55], v[156:159]\n"
    "ds_read_b128 a[184:187], v237 offset:60416\n"
    "v_exp_f32 v118, v118\n"
    "v_mfma_f32_16x16x32_bf16 v[252:255], v[200:203], v[28:31], v[252:255]\n"
    "v_exp_f32 v119, v119\n"
    "v_mfma_f32_16x16x32_bf16 v[156:159], a[120:123], a[56:59], v[156:159]\n"
    "ds_read_b128 a[188:191], v237 offset:60928\n"
    "v_cvt_pk_bf16_f32 v36, v116, v117\n"
    "v_mfma_f32_16x16x32_bf16 v[156:159], a[124:127], a[60:63], v[156:159]\n"
    "v_exp_f32 v124, v124\n"
    "s_waitcnt vmcnt(13) lgkmcnt(3)\n"
    "s_barrier\n"
    "v_mfma_f32_16x16x32_bf16 v[160:163], a[128:131], a[0:3], 0\n"
    "v_exp_f32 v125, v125\n"
    "v_mfma_f32_16x16x32_bf16 v[160:163], a[132:135], a[4:7], v[160:163]\n"
    "ds_read_b128 a[64:67], v238 offset:0\n"
    "v_cvt_pk_bf16_f32 v37, v118, v119\n"
    "v_mfma_f32_16x16x32_bf16 v[160:163], a[136:139], a[8:11], v[160:163]\n"
    "buffer_load_dwordx4 v225, s[20:23], s76 offen offset:1024 nt lds\n"
    "v_exp_f32 v126, v126\n"
    "v_mfma_f32_16x16x32_bf16 v[160:163], a[140:143], a[12:15], v[160:163]\n"
    "v_exp_f32 v127, v127\n"
    "v_mfma_f32_16x16x32_bf16 v[160:163], a[144:147], a[16:19], v[160:163]\n"
    "ds_read_b128 a[68:71], v238 offset:512\n"
    "v_cvt_pk_bf16_f32 v38, v124, v125\n"
    "v_mfma_f32_16x16x32_bf16 v[160:163], a[148:151], a[20:23], v[160:163]\n"
    "ds_read_b128 a[72:75], v238 offset:1024\n"
    "v_cvt_pk_bf16_f32 v39, v126, v127\n"
    "v_mfma_f32_16x16x32_bf16 v[160:163], a[152:155], a[24:27], v[160:163]\n"
    "v_exp_f32 v128, v128\n"
    "v_mfma_f32_16x16x32_bf16 v[160:163], a[156:159], a[28:31], v[160:163]\n"
    "ds_read_b128 a[76:79], v238 offset:1536\n"
    "v_exp_f32 v129, v129\n"
    "v_mfma_f32_16x16x32_bf16 v[164:167], a[128:131], a[32:35], 0\n"
    "buffer_load_dwordx4 v226, s[20:23], s76 offen offset:2048 nt lds\n"
    "v_exp_f32 v130, v130\n"
    "v_mfma_f32_16x16x32_bf16 v[164:167], a[132:135], a[36:39], v[164:167]\n"
    "v_exp_f32 v131, v131\n"
    "v_mfma_f32_16x16x32_bf16 v[164:167], a[136:139], a[40:43], v[164:167]\n"
    "ds_read_b128 a[80:83], v238 offset:2048\n"
    "v_cvt_pk_bf16_f32 v40, v128, v129\n"
    "v_mfma_f32_16x16x32_bf16 v[164:167], a[140:143], a[44:47], v[164:167]\n"
    "ds_read_b128 a[84:87], v238 offset:2560\n"
    "v_exp_f32 v136, v136\n"
    "v_mfma_f32_16x16x32_bf16 v[164:167], a[144:147], a[48:51], v[164:167]\n"
    "buffer_load_dwordx4 v227, s[20:23], s76 offen offset:3072 nt lds\n"
    "v_exp_f32 v137, v137\n"
    "v_mfma_f32_16x16x32_bf16 v[164:167], a[148:151], a[52:55], v[164:167]\n"
    "ds_read_b128 a[88:91], v238 offset:3072\n"
    "v_cvt_pk_bf16_f32 v41, v130, v131\n"
    "v_mfma_f32_16x16x32_bf16 v[204:207], v[200:203], v[32:35], v[204:207]\n"
    "v_exp_f32 v138, v138\n"
    "v_mfma_f32_16x16x32_bf16 v[164:167], a[152:155], a[56:59], v[164:167]\n"
    "v_exp_f32 v139, v139\n"
    "v_mfma_f32_16x16x32_bf16 v[164:167], a[156:159], a[60:63], v[164:167]\n"
    "ds_read_b128 a[92:95], v238 offset:3584\n"
    "v_cvt_pk_bf16_f32 v42, v136, v137\n"
    "v_mfma_f32_16x16x32_bf16 v[168:171], a[160:163], a[0:3], 0\n"
    "v_exp_f32 v132, v132\n"
    "v_mfma_f32_16x16x32_bf16 v[168:171], a[164:167], a[4:7], v[168:171]\n"
    "ds_read_b128 a[96:99], v238 offset:8192\n"
    "s_add_u32 m0, s33, 0xc000\n"
    "v_exp_f32 v133, v133\n"
    "v_mfma_f32_16x16x32_bf16 v[168:171], a[168:171], a[8:11], v[168:171]\n"
    "buffer_load_dwordx4 v224, s[20:23], s77 offen offset:0 nt lds\n"
    "v_cvt_pk_bf16_f32 v43, v138, v139\n"
    "v_mfma_f32_16x16x32_bf16 v[168:171], a[172:175], a[12:15], v[168:171]\n"
    "ds_read_b128 a[100:103], v238 offset:8704\n"
    "v_exp_f32 v134, v134\n"
    "v_mfma_f32_16x16x32_bf16 v[252:255], v[200:203], v[36:39], v[252:255]\n"
    "v_exp_f32 v135, v135\n"
    "v_mfma_f32_16x16x32_bf16 v[168:171], a[176:179], a[16:19], v[168:171]\n"
    "ds_read_b128 a[104:107], v238 offset:9216\n"
    "v_cvt_pk_bf16_f32 v44, v132, v133\n"
    "s_waitcnt lgkmcnt(4)\n"
    "v_mfma_f32_16x16x32_bf16 v[168:171], a[180:183], a[20:23], v[168:171]\n"
    "v_exp_f32 v140, v140\n"
    "v_mfma_f32_16x16x32_bf16 v[168:171], a[184:187], a[24:27], v[168:171]\n"
    "ds_read_b128 a[108:111], v238 offset:9728\n"
    "v_exp_f32 v141, v141\n"
    "v_mfma_f32_16x16x32_bf16 v[168:171], a[188:191], a[28:31], v[168:171]\n"
    "ds_read_b128 a[112:115], v238 offset:10240\n"
    "v_cvt_pk_bf16_f32 v45, v134, v135\n"
    "v_mfma_f32_16x16x32_bf16 v[172:175], a[160:163], a[32:35], 0\n"
    "buffer_load_dwordx4 v225, s[20:23], s77 offen offset:1024 nt lds\n"
    "v_exp_f32 v142, v142\n"
    "v_mfma_f32_16x16x32_bf16 v[172:175], a[164:167], a[36:39], v[172:175]\n"
    "v_exp_f32 v143, v143\n"
    "v_mfma_f32_16x16x32_bf16 v[172:175], a[168:171], a[40:43], v[172:175]\n"
    "ds_read_b128 a[116:119], v238 offset:10752\n"
    "v_cvt_pk_bf16_f32 v46, v140, v141\n"
    "v_mfma_f32_16x16x32_bf16 v[172:175], a[172:175], a[44:47], v[172:175]\n"
    "ds_read_b128 a[120:123], v238 offset:11264\n"
    "v_cvt_pk_bf16_f32 v47, v142, v143\n"
    "v_mfma_f32_16x16x32_bf16 v[172:175], a[176:179], a[48:51], v[172:175]\n"
    "v_exp_f32 v144, v144\n"
    "v_mfma_f32_16x16x32_bf16 v[172:175], a[180:183], a[52:55], v[172:175]\n"
    "v_exp_f32 v145, v145\n"
    "v_mfma_f32_16x16x32_bf16 v[172:175], a[184:187], a[56:59], v[172:175]\n"
    "ds_read_b128 a[124:127], v238 offset:11776\n"
    "v_exp_f32 v146, v146\n"
    "v_mfma_f32_16x16x32_bf16 v[172:175], a[188:191], a[60:63], v[172:175]\n"
    "s_waitcnt vmcnt(10) lgkmcnt(3)\n"
    "s_barrier\n"
    "v_mfma_f32_16x16x32_bf16 v[112:115], a[64:67], a[0:3], 0\n"
    "v_exp_f32 v147, v147\n"
    "v_mfma_f32_16x16x32_bf16 v[112:115], a[68:71], a[4:7], v[112:115]\n"
    "ds_read_b128 a[128:131], v238 offset:16384\n"
    "v_cvt_pk_bf16_f32 v48, v144, v145\n"
    "v_mfma_f32_16x16x32_bf16 v[112:115], a[72:75], a[8:11], v[112:115]\n"
    "buffer_load_dwordx4 v226, s[20:23], s77 offen offset:2048 nt lds\n"
    "v_exp_f32 v152, v152\n"
    "v_mfma_f32_16x16x32_bf16 v[112:115], a[76:79], a[12:15], v[112:115]\n"
    "ds_read_b128 a[132:135], v238 offset:16896\n"
    "v_exp_f32 v153, v153\n"
    "v_mfma_f32_16x16x32_bf16 v[204:207], v[200:203], v[40:43], v[204:207]\n"
    "ds_read_b128 a[136:139], v238 offset:17408\n"
    "v_cvt_pk_bf16_f32 v49, v146, v147\n"
    "v_mfma_f32_16x16x32_bf16 v[112:115], a[80:83], a[16:19], v[112:115]\n"
    "buffer_load_dwordx4 v227, s[20:23], s77 offen offset:3072 nt lds\n"
    "v_exp_f32 v154, v154\n"
    "v_mfma_f32_16x16x32_bf16 v[112:115], a[84:87], a[20:23], v[112:115]\n"
    "v_exp_f32 v155, v155\n"
    "v_mfma_f32_16x16x32_bf16 v[112:115], a[88:91], a[24:27], v[112:115]\n"
    "ds_read_b128 a[140:143], v238 offset:17920\n"
    "v_cvt_pk_bf16_f32 v50, v152, v153\n"
    "v_mfma_f32_16x16x32_bf16 v[112:115], a[92:95], a[28:31], v[112:115]\n"
    "v_exp_f32 v148, v148\n"
    "v_mfma_f32_16x16x32_bf16 v[116:119], a[64:67], a[32:35], 0\n"
    "v_exp_f32 v149, v149\n"
    "v_mfma_f32_16x16x32_bf16 v[116:119], a[68:71], a[36:39], v[116:119]\n"
    "ds_read_b128 a[144:147], v238 offset:18432\n"
    "v_cvt_pk_bf16_f32 v51, v154, v155\n"
    "v_mfma_f32_16x16x32_bf16 v[252:255], v[200:203], v[44:47], v[252:255]\n"
    "ds_read_b128 a[148:151], v238 offset:18944\n"
    "s_add_u32 m0, s33, 0x10000\n"
    "v_mfma_f32_16x16x32_bf16 v[116:119], a[72:75], a[40:43], v[116:119]\n"
    "buffer_load_dwordx4 v224, s[20:23], s78 offen offset:0 nt lds\n"
    "v_exp_f32 v150, v150\n"
    "v_mfma_f32_16x16x32_bf16 v[116:119], a[76:79], a[44:47], v[116:119]\n"
    "v_exp_f32 v151, v151\n"
    "v_mfma_f32_16x16x32_bf16 v[116:119], a[80:83], a[48:51], v[116:119]\n"
    "ds_read_b128 a[152:155], v238 offset:19456\n"
    "v_cvt_pk_bf16_f32 v52, v148, v149\n"
    "v_mfma_f32_16x16x32_bf16 v[116:119], a[84:87], a[52:55], v[116:119]\n"
    "v_exp_f32 v156, v156\n"
    "v_mfma_f32_16x16x32_bf16 v[116:119], a[88:91], a[56:59], v[116:119]\n"
    "buffer_load_dwordx4 v225, s[20:23], s78 offen offset:1024 nt lds\n"
    "v_exp_f32 v157, v157\n"
    "v_mfma_f32_16x16x32_bf16 v[116:119], a[92:95], a[60:63], v[116:119]\n"
    "ds_read_b128 a[156:159], v238 offset:19968\n"
    "v_cvt_pk_bf16_f32 v53, v150, v151\n"
    "v_mfma_f32_16x16x32_bf16 v[120:123], a[96:99], a[0:3], 0\n"
    "ds_read_b128 a[160:163], v238 offset:24576\n"
    "v_exp_f32 v158, v158\n"
    "v_mfma_f32_16x16x32_bf16 v[120:123], a[100:103], a[4:7], v[120:123]\n"
    "v_exp_f32 v159, v159\n"
    "v_mfma_f32_16x16x32_bf16 v[120:123], a[104:107], a[8:11], v[120:123]\n"
    "ds_read_b128 a[164:167], v238 offset:25088\n"
    "v_cvt_pk_bf16_f32 v54, v156, v157\n"
    "v_mfma_f32_16x16x32_bf16 v[120:123], a[108:111], a[12:15], v[120:123]\n"
    "ds_read_b128 a[168:171], v238 offset:25600\n"
    "v_cvt_pk_bf16_f32 v55, v158, v159\n"
    "v_mfma_f32_16x16x32_bf16 v[120:123], a[112:115], a[16:19], v[120:123]\n"
    "buffer_load_dwordx4 v226, s[20:23], s78 offen offset:2048 nt lds\n"
    "s_waitcnt lgkmcnt(4)\n"
    "v_mfma_f32_16x16x32_bf16 v[120:123], a[116:119], a[20:23], v[120:123]\n"
    "v_exp_f32 v160, v160\n"
    "v_mfma_f32_16x16x32_bf16 v[120:123], a[120:123], a[24:27], v[120:123]\n"
    "v_exp_f32 v161, v161\n"
    "v_mfma_f32_16x16x32_bf16 v[120:123], a[124:127], a[28:31], v[120:123]\n"
    "ds_read_b128 a[172:175], v238 offset:26112\n"
    "v_exp_f32 v162, v162\n"
    "v_mfma_f32_16x16x32_bf16 v[124:127], a[96:99], a[32:35], 0\n"
    "v_exp_f32 v163, v163\n"
    "v_mfma_f32_16x16x32_bf16 v[124:127], a[100:103], a[36:39], v[124:127]\n"
    "ds_read_b128 a[176:179], v238 offset:26624\n"
    "v_cvt_pk_bf16_f32 v56, v160, v161\n"
    "v_mfma_f32_16x16x32_bf16 v[124:127], a[104:107], a[40:43], v[124:127]\n"
    "buffer_load_dwordx4 v227, s[20:23], s78 offen offset:3072 nt lds\n"
    "v_mfma_f32_16x16x32_bf16 v[124:127], a[108:111], a[44:47], v[124:127]\n"
    "ds_read_b128 a[180:183], v238 offset:27136\n"
    "v_exp_f32 v168, v168\n"
    "v_mfma_f32_16x16x32_bf16 v[204:207], v[200:203], v[48:51], v[204:207]\n"
    "v_exp_f32 v169, v169\n"
    "v_mfma_f32_16x16x32_bf16 v[124:127], a[112:115], a[48:51], v[124:127]\n"
    "ds_read_b128 a[184:187], v238 offset:27648\n"
    "v_cvt_pk_bf16_f32 v57, v162, v163\n"
    "v_mfma_f32_16x16x32_bf16 v[124:127], a[116:119], a[52:55], v[124:127]\n"
    "v_exp_f32 v170, v170\n"
    "v_mfma_f32_16x16x32_bf16 v[124:127], a[120:123], a[56:59], v[124:127]\n"
    "v_exp_f32 v171, v171\n"
    "v_mfma_f32_16x16x32_bf16 v[124:127], a[124:127], a[60:63], v[124:127]\n"
    "ds_read_b128 a[188:191], v238 offset:28160\n"
    "v_cvt_pk_bf16_f32 v58, v168, v169\n"
    "v_mfma_f32_16x16x32_bf16 v[128:131], a[128:131], a[0:3], 0\n"
    "v_exp_f32 v164, v164\n"
    "v_mfma_f32_16x16x32_bf16 v[128:131], a[132:135], a[4:7], v[128:131]\n"
    "ds_read_b128 a[64:67], v236 offset:0\n"
    "v_exp_f32 v165, v165\n"
    "v_mfma_f32_16x16x32_bf16 v[252:255], v[200:203], v[52:55], v[252:255]\n"
    "ds_read_b128 a[68:71], v236 offset:512\n"
    "s_add_u32 m0, s33, 0x14000\n"
    "v_mfma_f32_16x16x32_bf16 v[128:131], a[136:139], a[8:11], v[128:131]\n"
    "buffer_load_dwordx4 v224, s[20:23], s79 offen offset:0 nt lds\n"
    "v_cvt_pk_bf16_f32 v59, v170, v171\n"
    "v_mfma_f32_16x16x32_bf16 v[128:131], a[140:143], a[12:15], v[128:131]\n"
    "v_exp_f32 v166, v166\n"
    "v_mfma_f32_16x16x32_bf16 v[128:131], a[144:147], a[16:19], v[128:131]\n"
    "ds_read_b128 a[72:75], v236 offset:1024\n"
    "v_exp_f32 v167, v167\n"
    "v_mfma_f32_16x16x32_bf16 v[128:131], a[148:151], a[20:23], v[128:131]\n"
    "ds_read_b128 a[76:79], v236 offset:1536\n"
    "v_cvt_pk_bf16_f32 v60, v164, v165\n"
    "v_mfma_f32_16x16x32_bf16 v[128:131], a[152:155], a[24:27], v[128:131]\n"
    "buffer_load_dwordx4 v225, s[20:23], s79 offen offset:1024 nt lds\n"
    "v_exp_f32 v172, v172\n"
    "s_waitcnt lgkmcnt(4)\n"
    "v_mfma_f32_16x16x32_bf16 v[128:131], a[156:159], a[28:31], v[128:131]\n"
    "v_exp_f32 v173, v173\n"
    "v_mfma_f32_16x16x32_bf16 v[132:135], a[128:131], a[32:35], 0\n"
    "ds_read_b128 a[80:83], v236 offset:2048\n"
    "v_cvt_pk_bf16_f32 v61, v166, v167\n"
    "v_mfma_f32_16x16x32_bf16 v[132:135], a[132:135], a[36:39], v[132:135]\n"
    "v_exp_f32 v174, v174\n"
    "v_mfma_f32_16x16x32_bf16 v[132:135], a[136:139], a[40:43], v[132:135]\n"
    "v_exp_f32 v175, v175\n"
    "v_mfma_f32_16x16x32_bf16 v[132:135], a[140:143], a[44:47], v[132:135]\n"
    "ds_read_b128 a[84:87], v236 offset:2560\n"
    "v_cvt_pk_bf16_f32 v62, v172, v173\n"
    "v_mfma_f32_16x16x32_bf16 v[132:135], a[144:147], a[48:51], v[132:135]\n"
    "buffer_load_dwordx4 v226, s[20:23], s79 offen offset:2048 nt lds\n"
    "v_mfma_f32_16x16x32_bf16 v[132:135], a[148:151], a[52:55], v[132:135]\n"
    "ds_read_b128 a[88:91], v236 offset:3072\n"
    "v_cvt_pk_bf16_f32 v63, v174, v175\n"
    "v_mfma_f32_16x16x32_bf16 v[132:135], a[152:155], a[56:59], v[132:135]\n"
    "v_exp_f32 v112, v112\n"
    "v_mfma_f32_16x16x32_bf16 v[132:135], a[156:159], a[60:63], v[132:135]\n"
    "ds_read_b128 a[92:95], v236 offset:3584\n"
    "v_exp_f32 v113, v113\n"
    "v_mfma_f32_16x16x32_bf16 v[136:139], a[160:163], a[0:3], 0\n"
    "buffer_load_dwordx4 v227, s[20:23], s79 offen offset:3072 nt lds\n"
    "v_mfma_f32_16x16x32_bf16 v[136:139], a[164:167], a[4:7], v[136:139]\n"
    "ds_read_b128 a[96:99], v236 offset:8192\n"
    "v_exp_f32 v114, v114\n"
    "v_mfma_f32_16x16x32_bf16 v[136:139], a[168:171], a[8:11], v[136:139]\n"
    "v_exp_f32 v115, v115\n"
    "v_mfma_f32_16x16x32_bf16 v[136:139], a[172:175], a[12:15], v[136:139]\n"
    "ds_read_b128 a[100:103], v236 offset:8704\n"
    "v_cvt_pk_bf16_f32 v64, v112, v113\n"
    "v_mfma_f32_16x16x32_bf16 v[136:139], a[176:179], a[16:19], v[136:139]\n"
    "v_exp_f32 v120, v120\n"
    "v_mfma_f32_16x16x32_bf16 v[136:139], a[180:183], a[20:23], v[136:139]\n"
    "v_exp_f32 v121, v121\n"
    "v_mfma_f32_16x16x32_bf16 v[204:207], v[200:203], v[56:59], v[204:207]\n"
    "ds_read_b128 a[104:107], v236 offset:9216\n"
    "s_add_u32 m0, s33, 0x18000\n"
    "v_mfma_f32_16x16x32_bf16 v[136:139], a[184:187], a[24:27], v[136:139]\n"
    "buffer_load_dwordx4 v228, s[24:27], s80 offen offset:0 nt lds\n"
    "v_mfma_f32_16x16x32_bf16 v[136:139], a[188:191], a[28:31], v[136:139]\n"
    "ds_read_b128 a[108:111], v236 offset:9728\n"
    "v_cvt_pk_bf16_f32 v65, v114, v115\n"
    "v_mfma_f32_16x16x32_bf16 v[140:143], a[160:163], a[32:35], 0\n"
    "v_exp_f32 v122, v122\n"
    "v_mfma_f32_16x16x32_bf16 v[140:143], a[164:167], a[36:39], v[140:143]\n"
    "ds_read_b128 a[112:115], v236 offset:10240\n"
    "v_exp_f32 v123, v123\n"
    "v_mfma_f32_16x16x32_bf16 v[140:143], a[168:171], a[40:43], v[140:143]\n"
    "ds_read_b128 a[116:119], v236 offset:10752\n"
    "v_cvt_pk_bf16_f32 v66, v120, v121\n"
    "v_mfma_f32_16x16x32_bf16 v[140:143], a[172:175], a[44:47], v[140:143]\n"
    "v_exp_f32 v116, v116\n"
    "v_mfma_f32_16x16x32_bf16 v[252:255], v[200:203], v[60:63], v[252:255]\n"
    "v_exp_f32 v117, v117\n"
    "v_mfma_f32_16x16x32_bf16 v[140:143], a[176:179], a[48:51], v[140:143]\n"
    "buffer_load_dwordx4 v229, s[24:27], s80 offen offset:1024 nt lds\n"
    "v_cvt_pk_bf16_f32 v67, v122, v123\n"
    "v_mfma_f32_16x16x32_bf16 v[140:143], a[180:183], a[52:55], v[140:143]\n"
    "ds_read_b128 a[120:123], v236 offset:11264\n"
    "v_exp_f32 v118, v118\n"
    "v_mfma_f32_16x16x32_bf16 v[140:143], a[184:187], a[56:59], v[140:143]\n"
    "ds_read_b128 a[124:127], v236 offset:11776\n"
    "v_exp_f32 v119, v119\n"
    "v_mfma_f32_16x16x32_bf16 v[140:143], a[188:191], a[60:63], v[140:143]\n"
    "s_waitcnt vmcnt(14) lgkmcnt(4)\n"
    "s_barrier\n"
    "v_mfma_f32_16x16x32_bf16 v[144:147], a[64:67], a[0:3], 0\n"
    "ds_read_b128 a[128:131], v236 offset:16384\n"
    "v_cvt_pk_bf16_f32 v68, v116, v117\n"
    "v_mfma_f32_16x16x32_bf16 v[144:147], a[68:71], a[4:7], v[144:147]\n"
    "v_exp_f32 v124, v124\n"
    "v_mfma_f32_16x16x32_bf16 v[144:147], a[72:75], a[8:11], v[144:147]\n"
    "buffer_load_dwordx4 v230, s[24:27], s80 offen offset:2048 nt lds\n"
    "v_mfma_f32_16x16x32_bf16 v[144:147], a[76:79], a[12:15], v[144:147]\n"
    "ds_read_b128 a[132:135], v236 offset:16896\n"
    "v_exp_f32 v125, v125\n"
    "v_mfma_f32_16x16x32_bf16 v[144:147], a[80:83], a[16:19], v[144:147]\n"
    "ds_read_b128 a[136:139], v236 offset:17408\n"
    "v_cvt_pk_bf16_f32 v69, v118, v119\n"
    "v_mfma_f32_16x16x32_bf16 v[144:147], a[84:87], a[20:23], v[144:147]\n"
    "v_exp_f32 v126, v126\n"
    "v_mfma_f32_16x16x32_bf16 v[144:147], a[88:91], a[24:27], v[144:147]\n"
    "v_exp_f32 v127, v127\n"
    "v_mfma_f32_16x16x32_bf16 v[144:147], a[92:95], a[28:31], v[144:147]\n"
    "ds_read_b128 a[140:143], v236 offset:17920\n"
    "v_cvt_pk_bf16_f32 v70, v124, v125\n"
    "v_mfma_f32_16x16x32_bf16 v[148:151], a[64:67], a[32:35], 0\n"
    "buffer_load_dwordx4 v231, s[24:27], s80 offen offset:3072 nt lds\n"
    "v_mfma_f32_16x16x32_bf16 v[148:151], a[68:71], a[36:39], v[148:151]\n"
    "ds_read_b128 a[144:147], v236 offset:18432\n"
    "v_cvt_pk_bf16_f32 v71, v126, v127\n"
    "v_mfma_f32_16x16x32_bf16 v[148:151], a[72:75], a[40:43], v[148:151]\n"
    "v_exp_f32 v128, v128\n"
    "v_mfma_f32_16x16x32_bf16 v[148:151], a[76:79], a[44:47], v[148:151]\n"
    "ds_read_b128 a[148:151], v236 offset:18944\n"
    "v_exp_f32 v129, v129\n"
    "v_mfma_f32_16x16x32_bf16 v[148:151], a[80:83], a[48:51], v[148:151]\n"
    "v_exp_f32 v130, v130\n"
    "v_mfma_f32_16x16x32_bf16 v[148:151], a[84:87], a[52:55], v[148:151]\n"
    "ds_read_b128 a[152:155], v236 offset:19456\n"
    "s_add_u32 m0, s33, 0x1c000\n"
    "v_mfma_f32_16x16x32_bf16 v[148:151], a[88:91], a[56:59], v[148:151]\n"
    "buffer_load_dwordx4 v228, s[24:27], s81 offen offset:0 nt lds\n"
    "v_mfma_f32_16x16x32_bf16 v[148:151], a[92:95], a[60:63], v[148:151]\n"
    "v_exp_f32 v131, v131\n"
    "v_mfma_f32_16x16x32_bf16 v[152:155], a[96:99], a[0:3], 0\n"
    "ds_read_b128 a[156:159], v236 offset:19968\n"
    "v_cvt_pk_bf16_f32 v72, v128, v129\n"
    "v_mfma_f32_16x16x32_bf16 v[152:155], a[100:103], a[4:7], v[152:155]\n"
    "v_exp_f32 v136, v136\n"
    "v_mfma_f32_16x16x32_bf16 v[204:207], v[200:203], v[64:67], v[204:207]\n"
    "ds_read_b128 a[160:163], v236 offset:24576\n"
    "v_exp_f32 v137, v137\n"
    "v_mfma_f32_16x16x32_bf16 v[152:155], a[104:107], a[8:11], v[152:155]\n"
    "ds_read_b128 a[164:167], v236 offset:25088\n"
    "v_cvt_pk_bf16_f32 v73, v130, v131\n"
    "v_mfma_f32_16x16x32_bf16 v[152:155], a[108:111], a[12:15], v[152:155]\n"
    "v_exp_f32 v138, v138\n"
    "s_waitcnt lgkmcnt(4)\n"
    "v_mfma_f32_16x16x32_bf16 v[152:155], a[112:115], a[16:19], v[152:155]\n"
    "buffer_load_dwordx4 v229, s[24:27], s81 offen offset:1024 nt lds\n"
    "v_mfma_f32_16x16x32_bf16 v[152:155], a[116:119], a[20:23], v[152:155]\n"
    "ds_read_b128 a[168:171], v236 offset:25600\n"
    "v_exp_f32 v139, v139\n"
    "v_mfma_f32_16x16x32_bf16 v[152:155], a[120:123], a[24:27], v[152:155]\n"
    "ds_read_b128 a[172:175], v236 offset:26112\n"
    "v_cvt_pk_bf16_f32 v74, v136, v137\n"
    "v_mfma_f32_16x16x32_bf16 v[152:155], a[124:127], a[28:31], v[152:155]\n"
    "v_exp_f32 v132, v132\n"
    "v_mfma_f32_16x16x32_bf16 v[252:255], v[200:203], v[68:71], v[252:255]\n"
    "v_exp_f32 v133, v133\n"
    "v_mfma_f32_16x16x32_bf16 v[156:159], a[96:99], a[32:35], 0\n"
    "buffer_load_dwordx4 v230, s[24:27], s81 offen offset:2048 nt lds\n"
    "v_mfma_f32_16x16x32_bf16 v[156:159], a[100:103], a[36:39], v[156:159]\n"
    "ds_read_b128 a[176:179], v236 offset:26624\n"
    "v_cvt_pk_bf16_f32 v75, v138, v139\n"
    "v_mfma_f32_16x16x32_bf16 v[156:159], a[104:107], a[40:43], v[156:159]\n"
    "ds_read_b128 a[180:183], v236 offset:27136\n"
    "v_exp_f32 v134, v134\n"
    "v_mfma_f32_16x16x32_bf16 v[156:159], a[108:111], a[44:47], v[156:159]\n"
    "v_exp_f32 v135, v135\n"
    "v_mfma_f32_16x16x32_bf16 v[156:159], a[112:115], a[48:51], v[156:159]\n"
    "ds_read_b128 a[184:187], v236 offset:27648\n"
    "v_cvt_pk_bf16_f32 v76, v132, v133\n"
    "v_mfma_f32_16x16x32_bf16 v[156:159], a[116:119], a[52:55], v[156:159]\n"
    "v_exp_f32 v140, v140\n"
    "v_mfma_f32_16x16x32_bf16 v[156:159], a[120:123], a[56:59], v[156:159]\n"
    "v_exp_f32 v141, v141\n"
    "v_mfma_f32_16x16x32_bf16 v[156:159], a[124:127], a[60:63], v[156:159]\n"
    "ds_read_b128 a[188:191], v236 offset:28160\n"
    "v_cvt_pk_bf16_f32 v77, v134, v135\n"
    "v_mfma_f32_16x16x32_bf16 v[112:115], a[128:131], a[0:3], 0\n"
    "v_exp_f32 v142, v142\n"
    "v_mfma_f32_16x16x32_bf16 v[112:115], a[132:135], a[4:7], v[112:115]\n"
    "ds_read_b128 a[64:67], v236 offset:32768\n"
    "v_exp_f32 v143, v143\n"
    "v_mfma_f32_16x16x32_bf16 v[112:115], a[136:139], a[8:11], v[112:115]\n"
    "buffer_load_dwordx4 v231, s[24:27], s81 offen offset:3072 nt lds\n"
    "v_mfma_f32_16x16x32_bf16 v[112:115], a[140:143], a[12:15], v[112:115]\n"
    "ds_read_b128 a[68:71], v236 offset:33280\n"
    "s_add_u32 m0, s33, 0x20000\n"
    "v_mfma_f32_16x16x32_bf16 v[112:115], a[144:147], a[16:19], v[112:115]\n"
    "buffer_load_dwordx4 v228, s[24:27], s82 offen offset:0 nt lds\n"
    "v_cvt_pk_bf16_f32 v78, v140, v141\n"
    "v_mfma_f32_16x16x32_bf16 v[112:115], a[148:151], a[20:23], v[112:115]\n"
    "ds_read_b128 a[72:75], v236 offset:33792\n"
    "v_cvt_pk_bf16_f32 v79, v142, v143\n"
    "s_waitcnt lgkmcnt(4)\n"
    "v_mfma_f32_16x16x32_bf16 v[112:115], a[152:155], a[24:27], v[112:115]\n"
    "v_exp_f32 v144, v144\n"
    "v_mfma_f32_16x16x32_bf16 v[112:115], a[156:159], a[28:31], v[112:115]\n"
    "ds_read_b128 a[76:79], v236 offset:34304\n"
    "v_mfma_f32_16x16x32_bf16 v[116:119], a[128:131], a[32:35], 0\n"
    "v_exp_f32 v145, v145\n"
    "v_mfma_f32_16x16x32_bf16 v[116:119], a[132:135], a[36:39], v[116:119]\n"
    "ds_read_b128 a[80:83], v236 offset:34816\n"
    "v_exp_f32 v146, v146\n"
    "v_mfma_f32_16x16x32_bf16 v[116:119], a[136:139], a[40:43], v[116:119]\n"
    "buffer_load_dwordx4 v229, s[24:27], s82 offen offset:1024 nt lds\n"
    "v_mfma_f32_16x16x32_bf16 v[116:119], a[140:143], a[44:47], v[116:119]\n"
    "v_exp_f32 v147, v147\n"
    "v_mfma_f32_16x16x32_bf16 v[116:119], a[144:147], a[48:51], v[116:119]\n"
    "ds_read_b128 a[84:87], v236 offset:35328\n"
    "v_cvt_pk_bf16_f32 v80, v144, v145\n"
    "v_mfma_f32_16x16x32_bf16 v[116:119], a[148:151], a[52:55], v[116:119]\n"
    "ds_read_b128 a[88:91], v236 offset:35840\n"
    "v_exp_f32 v152, v152\n"
    "v_mfma_f32_16x16x32_bf16 v[204:207], v[200:203], v[72:75], v[204:207]\n"
    "v_exp_f32 v153, v153\n"
    "v_mfma_f32_16x16x32_bf16 v[116:119], a[152:155], a[56:59], v[116:119]\n"
    "ds_read_b128 a[92:95], v236 offset:36352\n"
    "v_cvt_pk_bf16_f32 v81, v146, v147\n"
    "v_mfma_f32_16x16x32_bf16 v[116:119], a[156:159], a[60:63], v[116:119]\n"
    "v_exp_f32 v154, v154\n"
    "v_mfma_f32_16x16x32_bf16 v[120:123], a[160:163], a[0:3], 0\n"
    "buffer_load_dwordx4 v230, s[24:27], s82 offen offset:2048 nt lds\n"
    "v_mfma_f32_16x16x32_bf16 v[120:123], a[164:167], a[4:7], v[120:123]\n"
    "ds_read_b128 a[96:99], v236 offset:40960\n"
    "v_exp_f32 v155, v155\n"
    "v_mfma_f32_16x16x32_bf16 v[120:123], a[168:171], a[8:11], v[120:123]\n"
    "ds_read_b128 a[100:103], v236 offset:41472\n"
    "v_cvt_pk_bf16_f32 v82, v152, v153\n"
    "v_mfma_f32_16x16x32_bf16 v[120:123], a[172:175], a[12:15], v[120:123]\n"
    "v_exp_f32 v148, v148\n"
    "v_mfma_f32_16x16x32_bf16 v[252:255], v[200:203], v[76:79], v[252:255]\n"
    "v_exp_f32 v149, v149\n"
    "v_mfma_f32_16x16x32_bf16 v[120:123], a[176:179], a[16:19], v[120:123]\n"
    "ds_read_b128 a[104:107], v236 offset:41984\n"
    "v_cvt_pk_bf16_f32 v83, v154, v155\n"
    "v_mfma_f32_16x16x32_bf16 v[120:123], a[180:183], a[20:23], v[120:123]\n"
    "ds_read_b128 a[108:111], v236 offset:42496\n"
    "v_exp_f32 v150, v150\n"
    "v_mfma_f32_16x16x32_bf16 v[120:123], a[184:187], a[24:27], v[120:123]\n"
    "buffer_load_dwordx4 v231, s[24:27], s82 offen offset:3072 nt lds\n"
    "s_waitcnt lgkmcnt(4)\n"
    "v_mfma_f32_16x16x32_bf16 v[120:123], a[188:191], a[28:31], v[120:123]\n"
    "v_exp_f32 v151, v151\n"
    "v_mfma_f32_16x16x32_bf16 v[124:127], a[160:163], a[32:35], 0\n"
    "ds_read_b128 a[112:115], v236 offset:43008\n"
    "v_cvt_pk_bf16_f32 v84, v148, v149\n"
    "v_mfma_f32_16x16x32_bf16 v[124:127], a[164:167], a[36:39], v[124:127]\n"
    "ds_read_b128 a[116:119], v236 offset:43520\n"
    "s_add_u32 m0, s33, 0x24000\n"
    "v_mfma_f32_16x16x32_bf16 v[124:127], a[168:171], a[40:43], v[124:127]\n"
    "buffer_load_dwordx4 v228, s[24:27], s83 offen offset:0 nt lds\n"
    "PAD2_%=:\ns_cbranch_execz PADRET_%=\n"
    "v_mfma_f32_16x16x32_bf16 v[124:127], a[172:175], a[44:47], v[124:127]\n"
    "v_exp_f32 v156, v156\n"
    "v_mfma_f32_16x16x32_bf16 v[124:127], a[176:179], a[48:51], v[124:127]\n"
    "v_exp_f32 v157, v157\n"
    "v_mfma_f32_16x16x32_bf16 v[124:127], a[180:183], a[52:55], v[124:127]\n"
    "ds_read_b128 a[120:123], v236 offset:44032\n"
    "v_cvt_pk_bf16_f32 v85, v150, v151\n"
    "v_mfma_f32_16x16x32_bf16 v[124:127], a[184:187], a[56:59], v[124:127]\n"
    "ds_read_b128 a[124:127], v236 offset:44544\n"
    "v_exp_f32 v158, v158\n"
    "v_mfma_f32_16x16x32_bf16 v[124:127], a[188:191], a[60:63], v[124:127]\n"
    "s_waitcnt vmcnt(17) lgkmcnt(4)\n"
    "s_barrier\n"
    "v_mfma_f32_16x16x32_bf16 v[128:131], a[64:67], a[0:3], 0\n"
    "v_exp_f32 v159, v159\n"
    "v_mfma_f32_16x16x32_bf16 v[128:131], a[68:71], a[4:7], v[128:131]\n"
    "ds_read_b128 a[128:131], v236 offset:49152\n"
    "v_cvt_pk_bf16_f32 v86, v156, v157\n"
    "v_mfma_f32_16x16x32_bf16 v[128:131], a[72:75], a[8:11], v[128:131]\n"
    "buffer_load_dwordx4 v229, s[24:27], s83 offen offset:1024 nt lds\n"
    "v_mfma_f32_16x16x32_bf16 v[128:131], a[76:79], a[12:15], v[128:131]\n"
    "ds_read_b128 a[132:135], v236 offset:49664\n"
    "v_cvt_pk_bf16_f32 v87, v158, v159\n"
    "v_mfma_f32_16x16x32_bf16 v[128:131], a[80:83], a[16:19], v[128:131]\n"
    "v_exp_f32 v112, v112\n"
    "v_mfma_f32_16x16x32_bf16 v[128:131], a[84:87], a[20:23], v[128:131]\n"
    "ds_read_b128 a[136:139], v236 offset:50176\n"
    "v_exp_f32 v113, v113\n"
    "v_mfma_f32_16x16x32_bf16 v[128:131], a[88:91], a[24:27], v[128:131]\n"
    "buffer_load_dwordx4 v230, s[24:27], s83 offen offset:2048 nt lds\n"
    "v_mfma_f32_16x16x32_bf16 v[128:131], a[92:95], a[28:31], v[128:131]\n"
    "ds_read_b128 a[140:143], v236 offset:50688\n"
    "v_exp_f32 v114, v114\n"
    "v_mfma_f32_16x16x32_bf16 v[132:135], a[64:67], a[32:35], 0\n"
    "v_exp_f32 v115, v115\n"
    "v_mfma_f32_16x16x32_bf16 v[132:135], a[68:71], a[36:39], v[132:135]\n"
    "ds_read_b128 a[144:147], v236 offset:51200\n"
    "v_mfma_f32_16x16x32_bf16 v[132:135], a[72:75], a[40:43], v[132:135]\n"
    "ds_read_b128 a[148:151], v236 offset:51712\n"
    "v_cvt_pk_bf16_f32 v88, v112, v113\n"
    "v_mfma_f32_16x16x32_bf16 v[132:135], a[76:79], a[44:47], v[132:135]\n"
    "v_exp_f32 v120, v120\n"
    "v_mfma_f32_16x16x32_bf16 v[204:207], v[200:203], v[80:83], v[204:207]\n"
    "v_exp_f32 v121, v121\n"
    "v_mfma_f32_16x16x32_bf16 v[132:135], a[80:83], a[48:51], v[132:135]\n"
    "buffer_load_dwordx4 v231, s[24:27], s83 offen offset:3072 nt lds\n"
    "v_mfma_f32_16x16x32_bf16 v[132:135], a[84:87], a[52:55], v[132:135]\n"
    "ds_read_b128 a[152:155], v236 offset:52224\n"
    "v_cvt_pk_bf16_f32 v89, v114, v115\n"
    "v_mfma_f32_16x16x32_bf16 v[132:135], a[88:91], a[56:59], v[132:135]\n"
    "ds_read_b128 a[156:159], v236 offset:52736\n"
    "v_mfma_f32_16x16x32_bf16 v[132:135], a[92:95], a[60:63], v[132:135]\n"
    "v_exp_f32 v122, v122\n"
    "v_mfma_f32_16x16x32_bf16 v[136:139], a[96:99], a[0:3], 0\n"
    "v_exp_f32 v123, v123\n"
    "v_mfma_f32_16x16x32_bf16 v[136:139], a[100:103], a[4:7], v[136:139]\n"
    "ds_read_b128 a[160:163], v236 offset:57344\n"
    "v_cvt_pk_bf16_f32 v90, v120, v121\n"
    "v_mfma_f32_16x16x32_bf16 v[252:255], v[200:203], v[84:87], v[252:255]\n"
    "ds_read_b128 a[164:167], v236 offset:57856\n"
    "s_add_u32 m0, s33, 0x0\n"
    "v_mfma_f32_16x16x32_bf16 v[136:139], a[104:107], a[8:11], v[136:139]\n"
    "buffer_load_dwordx4 v228, s[24:27], s84 offen offset:0 nt lds\n"
    "v_mfma_f32_16x16x32_bf16 v[136:139], a[108:111], a[12:15], v[136:139]\n"
    "v_exp_f32 v116, v116\n"
    "s_waitcnt lgkmcnt(4)\n"
    "v_mfma_f32_16x16x32_bf16 v[136:139], a[112:115], a[16:19], v[136:139]\n"
    "ds_read_b128 a[168:171], v236 offset:58368\n"
    "v_exp_f32 v117, v117\n"
    "v_mfma_f32_16x16x32_bf16 v[136:139], a[116:119], a[20:23], v[136:139]\n"
    "ds_read_b128 a[172:175], v236 offset:58880\n"
    "v_cvt_pk_bf16_f32 v91, v122, v123\n"
    "v_mfma_f32_16x16x32_bf16 v[136:139], a[120:123], a[24:27], v[136:139]\n"
    "v_exp_f32 v118, v118\n"
    "v_mfma_f32_16x16x32_bf16 v[136:139], a[124:127], a[28:31], v[136:139]\n"
    "v_exp_f32 v119, v119\n"
    "v_mfma_f32_16x16x32_bf16 v[140:143], a[96:99], a[32:35], 0\n"
    "buffer_load_dwordx4 v229, s[24:27], s84 offen offset:1024 nt lds\n"
    "v_mfma_f32_16x16x32_bf16 v[140:143], a[100:103], a[36:39], v[140:143]\n"
    "ds_read_b128 a[176:179], v236 offset:59392\n"
    "v_cvt_pk_bf16_f32 v92, v116, v117\n"
    "v_mfma_f32_16x16x32_bf16 v[140:143], a[104:107], a[40:43], v[140:143]\n"
    "ds_read_b128 a[180:183], v236 offset:59904\n"
    "v_exp_f32 v124, v124\n"
    "v_mfma_f32_16x16x32_bf16 v[140:143], a[108:111], a[44:47], v[140:143]\n"
    "v_exp_f32 v125, v125\n"
    "v_mfma_f32_16x16x32_bf16 v[140:143], a[112:115], a[48:51], v[140:143]\n"
    "buffer_load_dwordx4 v230, s[24:27], s84 offen offset:2048 nt lds\n"
    "v_mfma_f32_16x16x32_bf16 v[140:143], a[116:119], a[52:55], v[140:143]\n"
    "ds_read_b128 a[184:187], v236 offset:60416\n"
    "v_cvt_pk_bf16_f32 v93, v118, v119\n"
    "v_mfma_f32_16x16x32_bf16 v[140:143], a[120:123], a[56:59], v[140:143]\n"
    "ds_read_b128 a[188:191], v236 offset:60928\n"
    "v_mfma_f32_16x16x32_bf16 v[140:143], a[124:127], a[60:63], v[140:143]\n"
    "v_exp_f32 v126, v126\n"
    "v_mfma_f32_16x16x32_bf16 v[144:147], a[128:131], a[0:3], 0\n"
    "v_exp_f32 v127, v127\n"
    "v_mfma_f32_16x16x32_bf16 v[144:147], a[132:135], a[4:7], v[144:147]\n"
    "ds_read_b128 a[64:67], v237 offset:0\n"
    "v_cvt_pk_bf16_f32 v94, v124, v125\n"
    "v_mfma_f32_16x16x32_bf16 v[144:147], a[136:139], a[8:11], v[144:147]\n"
    "buffer_load_dwordx4 v231, s[24:27], s84 offen offset:3072 nt lds\n"
    "v_mfma_f32_16x16x32_bf16 v[144:147], a[140:143], a[12:15], v[144:147]\n"
    "ds_read_b128 a[68:71], v237 offset:512\n"
    "v_cvt_pk_bf16_f32 v95, v126, v127\n"
    "v_mfma_f32_16x16x32_bf16 v[144:147], a[144:147], a[16:19], v[144:147]\n"
    "ds_read_b128 a[72:75], v237 offset:1024\n"
    "v_exp_f32 v128, v128\n"
    "v_mfma_f32_16x16x32_bf16 v[144:147], a[148:151], a[20:23], v[144:147]\n"
    "v_exp_f32 v129, v129\n"
    "s_waitcnt lgkmcnt(4)\n"
    "v_mfma_f32_16x16x32_bf16 v[144:147], a[152:155], a[24:27], v[144:147]\n"
    "ds_read_b128 a[76:79], v237 offset:1536\n"
    "v_exp_f32 v130, v130\n"
    "v_mfma_f32_16x16x32_bf16 v[144:147], a[156:159], a[28:31], v[144:147]\n"
    "ds_read_b128 a[80:83], v237 offset:2048\n"
    "s_add_u32 m0, s33, 0x4000\n"
    "v_mfma_f32_16x16x32_bf16 v[148:151], a[128:131], a[32:35], 0\n"
    "buffer_load_dwordx4 v228, s[24:27], s85 offen offset:0 nt lds\n"
    "v_mfma_f32_16x16x32_bf16 v[148:151], a[132:135], a[36:39], v[148:151]\n"
    "v_exp_f32 v131, v131\n"
    "v_mfma_f32_16x16x32_bf16 v[148:151], a[136:139], a[40:43], v[148:151]\n"
    "ds_read_b128 a[84:87], v237 offset:2560\n"
    "v_cvt_pk_bf16_f32 v96, v128, v129\n"
    "v_mfma_f32_16x16x32_bf16 v[148:151], a[140:143], a[44:47], v[148:151]\n"
    "v_exp_f32 v136, v136\n"
    "v_mfma_f32_16x16x32_bf16 v[204:207], v[200:203], v[88:91], v[204:207]\n"
    "ds_read_b128 a[88:91], v237 offset:3072\n"
    "v_mfma_f32_16x16x32_bf16 v[148:151], a[144:147], a[48:51], v[148:151]\n"
    "ds_read_b128 a[92:95], v237 offset:3584\n"
    "v_exp_f32 v137, v137\n"
    "v_mfma_f32_16x16x32_bf16 v[148:151], a[148:151], a[52:55], v[148:151]\n"
    "ds_read_b128 a[96:99], v237 offset:8192\n"
    "v_cvt_pk_bf16_f32 v97, v130, v131\n"
    "v_mfma_f32_16x16x32_bf16 v[148:151], a[152:155], a[56:59], v[148:151]\n"
    "buffer_load_dwordx4 v229, s[24:27], s85 offen offset:1024 nt lds\n"
    "v_mfma_f32_16x16x32_bf16 v[148:151], a[156:159], a[60:63], v[148:151]\n"
    "v_exp_f32 v138, v138\n"
    "v_mfma_f32_16x16x32_bf16 v[152:155], a[160:163], a[0:3], 0\n"
    "ds_read_b128 a[100:103], v237 offset:8704\n"
    "v_exp_f32 v139, v139\n"
    "v_mfma_f32_16x16x32_bf16 v[152:155], a[164:167], a[4:7], v[152:155]\n"
    "ds_read_b128 a[104:107], v237 offset:9216\n"
    "v_cvt_pk_bf16_f32 v98, v136, v137\n"
    "v_mfma_f32_16x16x32_bf16 v[252:255], v[200:203], v[92:95], v[252:255]\n"
    "v_exp_f32 v132, v132\n"
    "v_mfma_f32_16x16x32_bf16 v[152:155], a[168:171], a[8:11], v[152:155]\n"
    "ds_read_b128 a[108:111], v237 offset:9728\n"
    "v_mfma_f32_16x16x32_bf16 v[152:155], a[172:175], a[12:15], v[152:155]\n"
    "v_exp_f32 v133, v133\n"
    "v_mfma_f32_16x16x32_bf16 v[152:155], a[176:179], a[16:19], v[152:155]\n"
    "buffer_load_dwordx4 v230, s[24:27], s85 offen offset:2048 nt lds\n"
    "v_mfma_f32_16x16x32_bf16 v[152:155], a[180:183], a[20:23], v[152:155]\n"
    "ds_read_b128 a[112:115], v237 offset:10240\n"
    "v_cvt_pk_bf16_f32 v99, v138, v139\n"
    "v_mfma_f32_16x16x32_bf16 v[152:155], a[184:187], a[24:27], v[152:155]\n"
    "ds_read_b128 a[116:119], v237 offset:10752\n"
    "v_exp_f32 v134, v134\n"
    "s_waitcnt lgkmcnt(5)\n"
    "v_mfma_f32_16x16x32_bf16 v[152:155], a[188:191], a[28:31], v[152:155]\n"
    "v_exp_f32 v135, v135\n"
    "v_mfma_f32_16x16x32_bf16 v[156:159], a[160:163], a[32:35], 0\n"
    "ds_read_b128 a[120:123], v237 offset:11264\n"
    "v_mfma_f32_16x16x32_bf16 v[156:159], a[164:167], a[36:39], v[156:159]\n"
    "ds_read_b128 a[124:127], v237 offset:11776\n"
    "v_cvt_pk_bf16_f32 v100, v132, v133\n"
    "v_mfma_f32_16x16x32_bf16 v[156:159], a[168:171], a[40:43], v[156:159]\n"
    "buffer_load_dwordx4 v231, s[24:27], s85 offen offset:3072 nt lds\n"
    "v_mfma_f32_16x16x32_bf16 v[156:159], a[172:175], a[44:47], v[156:159]\n"
    "v_exp_f32 v140, v140\n"
    "v_mfma_f32_16x16x32_bf16 v[156:159], a[176:179], a[48:51], v[156:159]\n"
    "v_exp_f32 v141, v141\n"
    "v_mfma_f32_16x16x32_bf16 v[156:159], a[180:183], a[52:55], v[156:159]\n"
    "v_cvt_pk_bf16_f32 v101, v134, v135\n"
    "v_exp_f32 v142, v142\n"
    "v_mfma_f32_16x16x32_bf16 v[156:159], a[184:187], a[56:59], v[156:159]\n"
    "v_exp_f32 v143, v143\n"
    "v_mfma_f32_16x16x32_bf16 v[156:159], a[188:191], a[60:63], v[156:159]\n"
    "s_waitcnt vmcnt(20) lgkmcnt(0)\n"
    "s_barrier\n"
    "v_mfma_f32_16x16x32_bf16 v[160:163], a[64:67], a[0:3], 0\n"
    "ds_read_b128 a[128:131], v237 offset:16384\n"
    "v_cvt_pk_bf16_f32 v102, v140, v141\n"
    "v_mfma_f32_16x16x32_bf16 v[160:163], a[68:71], a[4:7], v[160:163]\n"
    "ds_read_b128 a[132:135], v237 offset:16896\n"
    "s_add_u32 m0, s33, 0x8000\n"
    "v_mfma_f32_16x16x32_bf16 v[160:163], a[72:75], a[8:11], v[160:163]\n"
    "buffer_load_dwordx4 v228, s[24:27], s86 offen offset:0 nt lds\n"
    "v_mfma_f32_16x16x32_bf16 v[160:163], a[76:79], a[12:15], v[160:163]\n"
    "ds_read_b128 a[136:139], v237 offset:17408\n"
    "v_cvt_pk_bf16_f32 v103, v142, v143\n"
    "v_mfma_f32_16x16x32_bf16 v[160:163], a[80:83], a[16:19], v[160:163]\n"
    "buffer_load_dwordx4 v229, s[24:27], s86 offen offset:1024 nt lds\n"
    "v_mfma_f32_16x16x32_bf16 v[160:163], a[84:87], a[20:23], v[160:163]\n"
    "v_exp_f32 v144, v144\n"
    "v_mfma_f32_16x16x32_bf16 v[160:163], a[88:91], a[24:27], v[160:163]\n"
    "v_exp_f32 v145, v145\n"
    "v_mfma_f32_16x16x32_bf16 v[160:163], a[92:95], a[28:31], v[160:163]\n"
    "ds_read_b128 a[140:143], v237 offset:17920\n"
    "v_exp_f32 v146, v146\n"
    "v_mfma_f32_16x16x32_bf16 v[164:167], a[64:67], a[32:35], 0\n"
    "v_exp_f32 v147, v147\n"
    "v_mfma_f32_16x16x32_bf16 v[164:167], a[68:71], a[36:39], v[164:167]\n"
    "ds_read_b128 a[144:147], v237 offset:18432\n"
    "v_cvt_pk_bf16_f32 v104, v144, v145\n"
    "v_mfma_f32_16x16x32_bf16 v[164:167], a[72:75], a[40:43], v[164:167]\n"
    "buffer_load_dwordx4 v230, s[24:27], s86 offen offset:2048 nt lds\n"
    "v_mfma_f32_16x16x32_bf16 v[164:167], a[76:79], a[44:47], v[164:167]\n"
    "ds_read_b128 a[148:151], v237 offset:18944\n"
    "v_exp_f32 v152, v152\n"
    "v_mfma_f32_16x16x32_bf16 v[204:207], v[200:203], v[96:99], v[204:207]\n"
    "v_exp_f32 v153, v153\n"
    "v_mfma_f32_16x16x32_bf16 v[164:167], a[80:83], a[48:51], v[164:167]\n"
    "ds_read_b128 a[152:155], v237 offset:19456\n"
    "v_cvt_pk_bf16_f32 v105, v146, v147\n"
    "v_mfma_f32_16x16x32_bf16 v[164:167], a[84:87], a[52:55], v[164:167]\n"
    "v_exp_f32 v154, v154\n"
    "v_mfma_f32_16x16x32_bf16 v[164:167], a[88:91], a[56:59], v[164:167]\n"
    "v_exp_f32 v155, v155\n"
    "v_mfma_f32_16x16x32_bf16 v[164:167], a[92:95], a[60:63], v[164:167]\n"
    "ds_read_b128 a[156:159], v237 offset:19968\n"
    "v_cvt_pk_bf16_f32 v106, v152, v153\n"
    "v_mfma_f32_16x16x32_bf16 v[168:171], a[96:99], a[0:3], 0\n"
    "buffer_load_dwordx4 v231, s[24:27], s86 offen offset:3072 nt lds\n"
    "v_mfma_f32_16x16x32_bf16 v[168:171], a[100:103], a[4:7], v[168:171]\n"
    "ds_read_b128 a[160:163], v237 offset:24576\n"
    "v_exp_f32 v148, v148\n"
    "v_mfma_f32_16x16x32_bf16 v[252:255], v[200:203], v[100:103], v[252:255]\n"
    "v_exp_f32 v149, v149\n"
    "v_mfma_f32_16x16x32_bf16 v[168:171], a[104:107], a[8:11], v[168:171]\n"
    "ds_read_b128 a[164:167], v237 offset:25088\n"
    "v_cvt_pk_bf16_f32 v107, v154, v155\n"
    "v_mfma_f32_16x16x32_bf16 v[168:171], a[108:111], a[12:15], v[168:171]\n"
    "v_exp_f32 v150, v150\n"
    "v_mfma_f32_16x16x32_bf16 v[168:171], a[112:115], a[16:19], v[168:171]\n"
    "ds_read_b128 a[168:171], v237 offset:25600\n"
    "v_exp_f32 v151, v151\n"
    "v_mfma_f32_16x16x32_bf16 v[168:171], a[116:119], a[20:23], v[168:171]\n"
    "ds_read_b128 a[172:175], v237 offset:26112\n"
    "s_add_u32 m0, s33, 0xc000\n"
    "v_mfma_f32_16x16x32_bf16 v[168:171], a[120:123], a[24:27], v[168:171]\n"
    "buffer_load_dwordx4 v228, s[24:27], s87 offen offset:0 nt lds\n"
    "v_mfma_f32_16x16x32_bf16 v[168:171], a[124:127], a[28:31], v[168:171]\n"
    "ds_read_b128 a[176:179], v237 offset:26624\n"
    "v_cvt_pk_bf16_f32 v108, v148, v149\n"
    "v_mfma_f32_16x16x32_bf16 v[172:175], a[96:99], a[32:35], 0\n"
    "v_exp_f32 v156, v156\n"
    "v_mfma_f32_16x16x32_bf16 v[172:175], a[100:103], a[36:39], v[172:175]\n"
    "v_exp_f32 v157, v157\n"
    "v_mfma_f32_16x16x32_bf16 v[172:175], a[104:107], a[40:43], v[172:175]\n"
    "ds_read_b128 a[180:183], v237 offset:27136\n"
    "v_cvt_pk_bf16_f32 v109, v150, v151\n"
    "v_mfma_f32_16x16x32_bf16 v[172:175], a[108:111], a[44:47], v[172:175]\n"
    "v_exp_f32 v158, v158\n"
    "v_mfma_f32_16x16x32_bf16 v[172:175], a[112:115], a[48:51], v[172:175]\n"
    "buffer_load_dwordx4 v229, s[24:27], s87 offen offset:1024 nt lds\n"
    "v_mfma_f32_16x16x32_bf16 v[172:175], a[116:119], a[52:55], v[172:175]\n"
    "v_exp_f32 v159, v159\n"
    "v_mfma_f32_16x16x32_bf16 v[172:175], a[120:123], a[56:59], v[172:175]\n"
    "ds_read_b128 a[184:187], v237 offset:27648\n"
    "v_cvt_pk_bf16_f32 v110, v156, v157\n"
    "v_mfma_f32_16x16x32_bf16 v[172:175], a[124:127], a[60:63], v[172:175]\n"
    "ds_read_b128 a[188:191], v237 offset:28160\n"
    "v_cvt_pk_bf16_f32 v111, v158, v159\n"
    "s_waitcnt lgkmcnt(3)\n"
    "v_mfma_f32_16x16x32_bf16 v[176:179], a[128:131], a[0:3], 0\n"
    "ds_read_b64_tr_b16 a[64:65], v244 offset:32768\n"
    "ds_read_b64_tr_b16 a[66:67], v244 offset:34816\n"
    "v_mfma_f32_16x16x32_bf16 v[176:179], a[132:135], a[4:7], v[176:179]\n"
    "ds_read_b64_tr_b16 a[68:69], v244 offset:36864\n"
    "ds_read_b64_tr_b16 a[70:71], v244 offset:38912\n"
    "v_mfma_f32_16x16x32_bf16 v[176:179], a[136:139], a[8:11], v[176:179]\n"
    "buffer_load_dwordx4 v230, s[24:27], s87 offen offset:2048 nt lds\n"
    "v_mfma_f32_16x16x32_bf16 v[176:179], a[140:143], a[12:15], v[176:179]\n"
    "ds_read_b64_tr_b16 a[72:73], v244 offset:40960\n"
    "ds_read_b64_tr_b16 a[74:75], v244 offset:43008\n"
    "v_mfma_f32_16x16x32_bf16 v[176:179], a[144:147], a[16:19], v[176:179]\n"
    "ds_read_b64_tr_b16 a[76:77], v244 offset:45056\n"
    "v_exp_f32 v160, v160\n"
    "v_mfma_f32_16x16x32_bf16 v[176:179], a[148:151], a[20:23], v[176:179]\n"
    "ds_read_b64_tr_b16 a[78:79], v244 offset:47104\n"
    "v_exp_f32 v161, v161\n"
    "v_mfma_f32_16x16x32_bf16 v[176:179], a[152:155], a[24:27], v[176:179]\n"
    "buffer_load_dwordx4 v231, s[24:27], s87 offen offset:3072 nt lds\n"
    "v_mfma_f32_16x16x32_bf16 v[176:179], a[156:159], a[28:31], v[176:179]\n"
    "ds_read_b64_tr_b16 a[80:81], v245 offset:32768\n"
    "v_exp_f32 v162, v162\n"
    "v_mfma_f32_16x16x32_bf16 v[180:183], a[128:131], a[32:35], 0\n"
    "ds_read_b64_tr_b16 a[82:83], v245 offset:34816\n"
    "v_exp_f32 v163, v163\n"
    "v_mfma_f32_16x16x32_bf16 v[180:183], a[132:135], a[36:39], v[180:183]\n"
    "ds_read_b64_tr_b16 a[84:85], v245 offset:36864\n"
    "v_cvt_pk_bf16_f32 v112, v160, v161\n"
    "v_mfma_f32_16x16x32_bf16 v[180:183], a[136:139], a[40:43], v[180:183]\n"
    "ds_read_b64_tr_b16 a[86:87], v245 offset:38912\n"
    "ds_read_b64_tr_b16 a[88:89], v245 offset:40960\n"
    "v_mfma_f32_16x16x32_bf16 v[180:183], a[140:143], a[44:47], v[180:183]\n"
    "ds_read_b64_tr_b16 a[90:91], v245 offset:43008\n"
    "v_exp_f32 v168, v168\n"
    "v_mfma_f32_16x16x32_bf16 v[204:207], v[200:203], v[104:107], v[204:207]\n"
    "ds_read_b64_tr_b16 a[92:93], v245 offset:45056\n"
    "s_add_u32 m0, s33, 0x10000\n"
    "v_mfma_f32_16x16x32_bf16 v[180:183], a[144:147], a[48:51], v[180:183]\n"
    "buffer_load_dwordx4 v228, s[24:27], s88 offen offset:0 nt lds\n"
    "v_exp_f32 v169, v169\n"
    "v_mfma_f32_16x16x32_bf16 v[180:183], a[148:151], a[52:55], v[180:183]\n"
    "ds_read_b64_tr_b16 a[94:95], v245 offset:47104\n"
    "v_cvt_pk_bf16_f32 v113, v162, v163\n"
    "v_mfma_f32_16x16x32_bf16 v[180:183], a[152:155], a[56:59], v[180:183]\n"
    "ds_read_b64_tr_b16 a[96:97], v246 offset:32768\n"
    "ds_read_b64_tr_b16 a[98:99], v246 offset:34816\n"
    "v_mfma_f32_16x16x32_bf16 v[180:183], a[156:159], a[60:63], v[180:183]\n"
    "ds_read_b64_tr_b16 a[100:101], v246 offset:36864\n"
    "v_exp_f32 v170, v170\n"
    "v_mfma_f32_16x16x32_bf16 v[184:187], a[160:163], a[0:3], 0\n"
    "buffer_load_dwordx4 v229, s[24:27], s88 offen offset:1024 nt lds\n"
    "v_exp_f32 v171, v171\n"
    "v_mfma_f32_16x16x32_bf16 v[184:187], a[164:167], a[4:7], v[184:187]\n"
    "ds_read_b64_tr_b16 a[102:103], v246 offset:38912\n"
    "ds_read_b64_tr_b16 a[104:105], v246 offset:40960\n"
    "v_mfma_f32_16x16x32_bf16 v[252:255], v[200:203], v[108:111], v[252:255]\n"
    "ds_read_b64_tr_b16 a[106:107], v246 offset:43008\n"
    "v_cvt_pk_bf16_f32 v114, v168, v169\n"
    "v_mfma_f32_16x16x32_bf16 v[184:187], a[168:171], a[8:11], v[184:187]\n"
    "ds_read_b64_tr_b16 a[108:109], v246 offset:45056\n"
    "v_exp_f32 v164, v164\n"
    "v_mfma_f32_16x16x32_bf16 v[184:187], a[172:175], a[12:15], v[184:187]\n"
    "ds_read_b64_tr_b16 a[110:111], v246 offset:47104\n"
    "v_exp_f32 v165, v165\n"
    "v_mfma_f32_16x16x32_bf16 v[184:187], a[176:179], a[16:19], v[184:187]\n"
    "ds_read_b64_tr_b16 a[112:113], v247 offset:32768\n"
    "v_cvt_pk_bf16_f32 v115, v170, v171\n"
    "s_waitcnt lgkmcnt(7)\n"
    "v_mfma_f32_16x16x32_bf16 v[184:187], a[180:183], a[20:23], v[184:187]\n"
    "ds_read_b64_tr_b16 a[114:115], v247 offset:34816\n"
    "ds_read_b64_tr_b16 a[116:117], v247 offset:36864\n"
    "v_mfma_f32_16x16x32_bf16 v[184:187], a[184:187], a[24:27], v[184:187]\n"
    "buffer_load_dwordx4 v230, s[24:27], s88 offen offset:2048 nt lds\n"
    "v_exp_f32 v166, v166\n"
    "v_mfma_f32_16x16x32_bf16 v[184:187], a[188:191], a[28:31], v[184:187]\n"
    "ds_read_b64_tr_b16 a[118:119], v247 offset:38912\n"
    "ds_read_b64_tr_b16 a[120:121], v247 offset:40960\n"
    "v_mfma_f32_16x16x32_bf16 v[188:191], a[160:163], a[32:35], 0\n"
    "ds_read_b64_tr_b16 a[122:123], v247 offset:43008\n"
    "v_exp_f32 v167, v167\n"
    "v_mfma_f32_16x16x32_bf16 v[188:191], a[164:167], a[36:39], v[188:191]\n"
    "ds_read_b64_tr_b16 a[124:125], v247 offset:45056\n"
    "v_cvt_pk_bf16_f32 v116, v164, v165\n"
    "v_mfma_f32_16x16x32_bf16 v[188:191], a[168:171], a[40:43], v[188:191]\n"
    "ds_read_b64_tr_b16 a[126:127], v247 offset:47104\n"
    "v_exp_f32 v172, v172\n"
    "v_mfma_f32_16x16x32_bf16 v[188:191], a[172:175], a[44:47], v[188:191]\n"
    "v_exp_f32 v173, v173\n"
    "v_mfma_f32_16x16x32_bf16 v[188:191], a[176:179], a[48:51], v[188:191]\n"
    "buffer_load_dwordx4 v231, s[24:27], s88 offen offset:3072 nt lds\n"
    "v_cvt_pk_bf16_f32 v117, v166, v167\n"
    "v_mfma_f32_16x16x32_bf16 v[188:191], a[180:183], a[52:55], v[188:191]\n"
    "v_exp_f32 v174, v174\n"
    "v_mfma_f32_16x16x32_bf16 v[188:191], a[184:187], a[56:59], v[188:191]\n"
    "v_exp_f32 v175, v175\n"
    "v_mfma_f32_16x16x32_bf16 v[188:191], a[188:191], a[60:63], v[188:191]\n"
    "s_waitcnt vmcnt(24) lgkmcnt(0)\n"
    "s_barrier\n"
    "v_mfma_f32_16x16x32_bf16 v[128:131], a[64:67], v[0:3], 0\n"
    "ds_read_b64_tr_b16 a[128:129], v244 offset:49152\n"
    "v_cvt_pk_bf16_f32 v118, v172, v173\n"
    "v_mfma_f32_16x16x32_bf16 v[128:131], a[68:71], v[8:11], v[128:131]\n"
    "ds_read_b64_tr_b16 a[130:131], v244 offset:51200\n"
    "s_add_u32 m0, s33, 0x14000\n"
    "v_mfma_f32_16x16x32_bf16 v[128:131], a[72:75], v[16:19], v[128:131]\n"
    "buffer_load_dwordx4 v228, s[24:27], s89 offen offset:0 nt lds\n"
    "ds_read_b64_tr_b16 a[132:133], v244 offset:53248\n"
    "v_mfma_f32_16x16x32_bf16 v[128:131], a[76:79], v[24:27], v[128:131]\n"
    "ds_read_b64_tr_b16 a[134:135], v244 offset:55296\n"
    "v_cvt_pk_bf16_f32 v119, v174, v175\n"
    "v_mfma_f32_16x16x32_bf16 v[132:135], a[64:67], v[4:7], 0\n"
    "ds_read_b64_tr_b16 a[136:137], v244 offset:57344\n"
    "v_exp_f32 v176, v176\n"
    "v_mfma_f32_16x16x32_bf16 v[132:135], a[68:71], v[12:15], v[132:135]\n"
    "ds_read_b64_tr_b16 a[138:139], v244 offset:59392\n"
    "v_exp_f32 v177, v177\n"
    "v_mfma_f32_16x16x32_bf16 v[132:135], a[72:75], v[20:23], v[132:135]\n"
    "buffer_load_dwordx4 v229, s[24:27], s89 offen offset:1024 nt lds\n"
    "v_mfma_f32_16x16x32_bf16 v[132:135], a[76:79], v[28:31], v[132:135]\n"
    "ds_read_b64_tr_b16 a[140:141], v244 offset:61440\n"
    "v_exp_f32 v178, v178\n"
    "v_mfma_f32_16x16x32_bf16 v[136:139], a[80:83], v[0:3], 0\n"
    "ds_read_b64_tr_b16 a[142:143], v244 offset:63488\n"
    "v_exp_f32 v179, v179\n"
    "v_mfma_f32_16x16x32_bf16 v[136:139], a[84:87], v[8:11], v[136:139]\n"
    "ds_read_b64_tr_b16 a[144:145], v245 offset:49152\n"
    "ds_read_b64_tr_b16 a[146:147], v245 offset:51200\n"
    "v_mfma_f32_16x16x32_bf16 v[136:139], a[88:91], v[16:19], v[136:139]\n"
    "ds_read_b64_tr_b16 a[148:149], v245 offset:53248\n"
    "v_cvt_pk_bf16_f32 v120, v176, v177\n"
    "v_mfma_f32_16x16x32_bf16 v[136:139], a[92:95], v[24:27], v[136:139]\n"
    "ds_read_b64_tr_b16 a[150:151], v245 offset:55296\n"
    "v_exp_f32 v184, v184\n"
    "v_mfma_f32_16x16x32_bf16 v[204:207], v[200:203], v[112:115], v[204:207]\n"
    "ds_read_b64_tr_b16 a[152:153], v245 offset:57344\n"
    "v_exp_f32 v185, v185\n"
    "v_mfma_f32_16x16x32_bf16 v[140:143], a[80:83], v[4:7], 0\n"
    "buffer_load_dwordx4 v230, s[24:27], s89 offen offset:2048 nt lds\n"
    "v_mfma_f32_16x16x32_bf16 v[140:143], a[84:87], v[12:15], v[140:143]\n"
    "ds_read_b64_tr_b16 a[154:155], v245 offset:59392\n"
    "v_cvt_pk_bf16_f32 v121, v178, v179\n"
    "v_mfma_f32_16x16x32_bf16 v[140:143], a[88:91], v[20:23], v[140:143]\n"
    "ds_read_b64_tr_b16 a[156:157], v245 offset:61440\n"
    "v_exp_f32 v186, v186\n"
    "v_mfma_f32_16x16x32_bf16 v[140:143], a[92:95], v[28:31], v[140:143]\n"
    "ds_read_b64_tr_b16 a[158:159], v245 offset:63488\n"
    "v_exp_f32 v187, v187\n"
    "v_mfma_f32_16x16x32_bf16 v[144:147], a[96:99], v[0:3], 0\n"
    "buffer_load_dwordx4 v231, s[24:27], s89 offen offset:3072 nt lds\n"
    "v_mfma_f32_16x16x32_bf16 v[144:147], a[100:103], v[8:11], v[144:147]\n"
    "ds_read_b64_tr_b16 a[160:161], v246 offset:49152\n"
    "ds_read_b64_tr_b16 a[162:163], v246 offset:51200\n"
    "v_mfma_f32_16x16x32_bf16 v[252:255], v[200:203], v[116:119], v[252:255]\n"
    "ds_read_b64_tr_b16 a[164:165], v246 offset:53248\n"
    "v_cvt_pk_bf16_f32 v122, v184, v185\n"
    "v_mfma_f32_16x16x32_bf16 v[144:147], a[104:107], v[16:19], v[144:147]\n"
    "ds_read_b64_tr_b16 a[166:167], v246 offset:55296\n"
    "v_exp_f32 v180, v180\n"
    "v_mfma_f32_16x16x32_bf16 v[144:147], a[108:111], v[24:27], v[144:147]\n"
    "ds_read_b64_tr_b16 a[168:169], v246 offset:57344\n"
    "v_exp_f32 v181, v181\n"
    "v_mfma_f32_16x16x32_bf16 v[148:151], a[96:99], v[4:7], 0\n"
    "ds_read_b64_tr_b16 a[170:171], v246 offset:59392\n"
    "v_cvt_pk_bf16_f32 v123, v186, v187\n"
    "v_mfma_f32_16x16x32_bf16 v[148:151], a[100:103], v[12:15], v[148:151]\n"
    "ds_read_b64_tr_b16 a[172:173], v246 offset:61440\n"
    "s_add_u32 m0, s33, 0x18000\n"
    "v_mfma_f32_16x16x32_bf16 v[148:151], a[104:107], v[20:23], v[148:151]\n"
    "buffer_load_dwordx4 v228, s[24:27], s90 offen offset:0 nt lds\n"
    "v_mfma_f32_16x16x32_bf16 v[148:151], a[108:111], v[28:31], v[148:151]\n"
    "ds_read_b64_tr_b16 a[174:175], v246 offset:63488\n"
    "v_exp_f32 v182, v182\n"
    "v_mfma_f32_16x16x32_bf16 v[152:155], a[112:115], v[0:3], 0\n"
    "ds_read_b64_tr_b16 a[176:177], v247 offset:49152\n"
    "v_exp_f32 v183, v183\n"
    "v_mfma_f32_16x16x32_bf16 v[152:155], a[116:119], v[8:11], v[152:155]\n"
    "ds_read_b64_tr_b16 a[178:179], v247 offset:51200\n"
    "v_cvt_pk_bf16_f32 v124, v180, v181\n"
    "v_mfma_f32_16x16x32_bf16 v[152:155], a[120:123], v[16:19], v[152:155]\n"
    "ds_read_b64_tr_b16 a[180:181], v247 offset:53248\n"
    "v_exp_f32 v188, v188\n"
    "v_mfma_f32_16x16x32_bf16 v[152:155], a[124:127], v[24:27], v[152:155]\n"
    "ds_read_b64_tr_b16 a[182:183], v247 offset:55296\n"
    "v_exp_f32 v189, v189\n"
    "v_mfma_f32_16x16x32_bf16 v[156:159], a[112:115], v[4:7], 0\n"
    "buffer_load_dwordx4 v229, s[24:27], s90 offen offset:1024 nt lds\n"
    "v_mfma_f32_16x16x32_bf16 v[156:159], a[116:119], v[12:15], v[156:159]\n"
    "ds_read_b64_tr_b16 a[184:185], v247 offset:57344\n"
    "ds_read_b64_tr_b16 a[186:187], v247 offset:59392\n"
    "v_mfma_f32_16x16x32_bf16 v[156:159], a[120:123], v[20:23], v[156:159]\n"
    "ds_read_b64_tr_b16 a[188:189], v247 offset:61440\n"
    "v_cvt_pk_bf16_f32 v125, v182, v183\n"
    "v_mfma_f32_16x16x32_bf16 v[156:159], a[124:127], v[28:31], v[156:159]\n"
    "ds_read_b64_tr_b16 a[190:191], v247 offset:63488\n"
    "v_exp_f32 v190, v190\n"
    "s_waitcnt lgkmcnt(7)\n"
    "v_mfma_f32_16x16x32_bf16 v[128:131], a[128:131], v[32:35], v[128:131]\n"
    "ds_read_b64_tr_b16 a[64:65], v248 offset:0\n"
    "v_exp_f32 v191, v191\n"
    "v_mfma_f32_16x16x32_bf16 v[128:131], a[132:135], v[40:43], v[128:131]\n"
    "ds_read_b64_tr_b16 a[66:67], v248 offset:2048\n"
    "v_cvt_pk_bf16_f32 v126, v188, v189\n"
    "v_mfma_f32_16x16x32_bf16 v[128:131], a[136:139], v[48:51], v[128:131]\n"
    "buffer_load_dwordx4 v230, s[24:27], s90 offen offset:2048 nt lds\n"
    "v_mfma_f32_16x16x32_bf16 v[128:131], a[140:143], v[56:59], v[128:131]\n"
    "ds_read_b64_tr_b16 a[68:69], v248 offset:4096\n"
    "v_cvt_pk_bf16_f32 v127, v190, v191\n"
    "v_mfma_f32_16x16x32_bf16 v[132:135], a[128:131], v[36:39], v[132:135]\n"
    "ds_read_b64_tr_b16 a[70:71], v248 offset:6144\n"
    "ds_read_b64_tr_b16 a[72:73], v248 offset:8192\n"
    "v_mfma_f32_16x16x32_bf16 v[132:135], a[132:135], v[44:47], v[132:135]\n"
    "ds_read_b64_tr_b16 a[74:75], v248 offset:10240\n"
    "ds_read_b64_tr_b16 a[76:77], v248 offset:12288\n"
    "v_mfma_f32_16x16x32_bf16 v[132:135], a[136:139], v[52:55], v[132:135]\n"
    "buffer_load_dwordx4 v231, s[24:27], s90 offen offset:3072 nt lds\n"
    "v_mfma_f32_16x16x32_bf16 v[132:135], a[140:143], v[60:63], v[132:135]\n"
    "ds_read_b64_tr_b16 a[78:79], v248 offset:14336\n"
    "ds_read_b64_tr_b16 a[80:81], v249 offset:0\n"
    "v_mfma_f32_16x16x32_bf16 v[136:139], a[144:147], v[32:35], v[136:139]\n"
    "ds_read_b64_tr_b16 a[82:83], v249 offset:2048\n"
    "v_mfma_f32_16x16x32_bf16 v[136:139], a[148:151], v[40:43], v[136:139]\n"
    "ds_read_b64_tr_b16 a[84:85], v249 offset:4096\n"
    "v_mfma_f32_16x16x32_bf16 v[136:139], a[152:155], v[48:51], v[136:139]\n"
    "ds_read_b64_tr_b16 a[86:87], v249 offset:6144\n"
    "v_mfma_f32_16x16x32_bf16 v[136:139], a[156:159], v[56:59], v[136:139]\n"
    "ds_read_b64_tr_b16 a[88:89], v249 offset:8192\n"
    "v_mfma_f32_16x16x32_bf16 v[204:207], v[200:203], v[120:123], v[204:207]\n"
    "ds_read_b64_tr_b16 a[90:91], v249 offset:10240\n"
    "v_mfma_f32_16x16x32_bf16 v[140:143], a[144:147], v[36:39], v[140:143]\n"
    "ds_read_b64_tr_b16 a[92:93], v249 offset:12288\n"
    "v_mfma_f32_16x16x32_bf16 v[140:143], a[148:151], v[44:47], v[140:143]\n"
    "ds_read_b64_tr_b16 a[94:95], v249 offset:14336\n"
    "ds_read_b64_tr_b16 a[96:97], v250 offset:0\n"
    "v_mfma_f32_16x16x32_bf16 v[140:143], a[152:155], v[52:55], v[140:143]\n"
    "ds_read_b64_tr_b16 a[98:99], v250 offset:2048\n"
    "v_mfma_f32_16x16x32_bf16 v[140:143], a[156:159], v[60:63], v[140:143]\n"
    "ds_read_b64_tr_b16 a[100:101], v250 offset:4096\n"
    "v_mfma_f32_16x16x32_bf16 v[144:147], a[160:163], v[32:35], v[144:147]\n"
    "ds_read_b64_tr_b16 a[102:103], v250 offset:6144\n"
    "v_mfma_f32_16x16x32_bf16 v[144:147], a[164:167], v[40:43], v[144:147]\n"
    "ds_read_b64_tr_b16 a[104:105], v250 offset:8192\n"
    "v_mfma_f32_16x16x32_bf16 v[252:255], v[200:203], v[124:127], v[252:255]\n"
    "ds_read_b64_tr_b16 a[106:107], v250 offset:10240\n"
    "v_mfma_f32_16x16x32_bf16 v[144:147], a[168:171], v[48:51], v[144:147]\n"
    "ds_read_b64_tr_b16 a[108:109], v250 offset:12288\n"
    "v_mfma_f32_16x16x32_bf16 v[144:147], a[172:175], v[56:59], v[144:147]\n"
    "ds_read_b64_tr_b16 a[110:111], v250 offset:14336\n"
    "v_mfma_f32_16x16x32_bf16 v[148:151], a[160:163], v[36:39], v[148:151]\n"
    "ds_read_b64_tr_b16 a[112:113], v251 offset:0\n"
    "v_mfma_f32_16x16x32_bf16 v[148:151], a[164:167], v[44:47], v[148:151]\n"
    "ds_read_b64_tr_b16 a[114:115], v251 offset:2048\n"
    "v_mfma_f32_16x16x32_bf16 v[148:151], a[168:171], v[52:55], v[148:151]\n"
    "ds_read_b64_tr_b16 a[116:117], v251 offset:4096\n"
    "ds_read_b64_tr_b16 a[118:119], v251 offset:6144\n"
    "v_mfma_f32_16x16x32_bf16 v[148:151], a[172:175], v[60:63], v[148:151]\n"
    "ds_read_b64_tr_b16 a[120:121], v251 offset:8192\n"
    "s_waitcnt lgkmcnt(12)\n"
    "v_mfma_f32_16x16x32_bf16 v[152:155], a[176:179], v[32:35], v[152:155]\n"
    "ds_read_b64_tr_b16 a[122:123], v251 offset:10240\n"
    "v_mfma_f32_16x16x32_bf16 v[152:155], a[180:183], v[40:43], v[152:155]\n"
    "ds_read_b64_tr_b16 a[124:125], v251 offset:12288\n"
    "v_mfma_f32_16x16x32_bf16 v[152:155], a[184:187], v[48:51], v[152:155]\n"
    "ds_read_b64_tr_b16 a[126:127], v251 offset:14336\n"
    "v_mfma_f32_16x16x32_bf16 v[152:155], a[188:191], v[56:59], v[152:155]\n"
    "v_mfma_f32_16x16x32_bf16 v[156:159], a[176:179], v[36:39], v[156:159]\n"
    "v_mfma_f32_16x16x32_bf16 v[156:159], a[180:183], v[44:47], v[156:159]\n"
    "v_mfma_f32_16x16x32_bf16 v[156:159], a[184:187], v[52:55], v[156:159]\n"
    "v_mfma_f32_16x16x32_bf16 v[156:159], a[188:191], v[60:63], v[156:159]\n"
    "s_waitcnt vmcnt(24) lgkmcnt(0)\n"
    "s_barrier\n"
    "v_mfma_f32_16x16x32_bf16 v[128:131], a[64:67], v[64:67], v[128:131]\n"
    "ds_read_b64_tr_b16 a[128:129], v248 offset:16384\n"
    "ds_read_b64_tr_b16 a[130:131], v248 offset:18432\n"
    "v_mfma_f32_16x16x32_bf16 v[128:131], a[68:71], v[72:75], v[128:131]\n"
    "ds_read_b64_tr_b16 a[132:133], v248 offset:20480\n"
    "s_add_u32 m0, s33, 0x1c000\n"
    "v_mfma_f32_16x16x32_bf16 v[128:131], a[72:75], v[80:83], v[128:131]\n"
    "buffer_load_dwordx4 v228, s[24:27], s91 offen offset:0 nt lds\n"
    "v_mfma_f32_16x16x32_bf16 v[128:131], a[76:79], v[88:91], v[128:131]\n"
    "ds_read_b64_tr_b16 a[134:135], v248 offset:22528\n"
    "ds_read_b64_tr_b16 a[136:137], v248 offset:24576\n"
    "v_mfma_f32_16x16x32_bf16 v[132:135], a[64:67], v[68:71], v[132:135]\n"
    "buffer_load_dwordx4 v229, s[24:27], s91 offen offset:1024 nt lds\n"
    "v_mfma_f32_16x16x32_bf16 v[132:135], a[68:71], v[76:79], v[132:135]\n"
    "ds_read_b64_tr_b16 a[138:139], v248 offset:26624\n"
    "ds_read_b64_tr_b16 a[140:141], v248 offset:28672\n"
    "v_mfma_f32_16x16x32_bf16 v[132:135], a[72:75], v[84:87], v[132:135]\n"
    "buffer_load_dwordx4 v230, s[24:27], s91 offen offset:2048 nt lds\n"
    "v_mfma_f32_16x16x32_bf16 v[132:135], a[76:79], v[92:95], v[132:135]\n"
    "ds_read_b64_tr_b16 a[142:143], v248 offset:30720\n"
    "ds_read_b64_tr_b16 a[144:145], v249 offset:16384\n"
    "v_mfma_f32_16x16x32_bf16 v[136:139], a[80:83], v[64:67], v[136:139]\n"
    "buffer_load_dwordx4 v231, s[24:27], s91 offen offset:3072 nt lds\n"
    "v_mfma_f32_16x16x32_bf16 v[136:139], a[84:87], v[72:75], v[136:139]\n"
    "ds_read_b64_tr_b16 a[146:147], v249 offset:18432\n"
    "s_add_u32 m0, s33, 0x20000\n"
    "PAD3_%=:\ns_cbranch_execz PADRET_%=\n"
    "v_mfma_f32_16x16x32_bf16 v[136:139], a[88:91], v[80:83], v[136:139]\n"
    "buffer_load_dwordx4 v228, s[24:27], s92 offen offset:0 nt lds\n"
    "v_mfma_f32_16x16x32_bf16 v[136:139], a[92:95], v[88:91], v[136:139]\n"
    "ds_read_b64_tr_b16 a[148:149], v249 offset:20480\n"
    "ds_read_b64_tr_b16 a[150:151], v249 offset:22528\n"
    "v_mfma_f32_16x16x32_bf16 v[140:143], a[80:83], v[68:71], v[140:143]\n"
    "buffer_load_dwordx4 v229, s[24:27], s92 offen offset:1024 nt lds\n"
    "v_mfma_f32_16x16x32_bf16 v[140:143], a[84:87], v[76:79], v[140:143]\n"
    "ds_read_b64_tr_b16 a[152:153], v249 offset:24576\n"
    "ds_read_b64_tr_b16 a[154:155], v249 offset:26624\n"
    "v_mfma_f32_16x16x32_bf16 v[140:143], a[88:91], v[84:87], v[140:143]\n"
    "ds_read_b64_tr_b16 a[156:157], v249 offset:28672\n"
    "v_rcp_f32 v216, v204\n"
    "v_mfma_f32_16x16x32_bf16 v[140:143], a[92:95], v[92:95], v[140:143]\n"
    "ds_read_b64_tr_b16 a[158:159], v249 offset:30720\n"
    "ds_read_b64_tr_b16 a[160:161], v250 offset:16384\n"
    "v_mfma_f32_16x16x32_bf16 v[144:147], a[96:99], v[64:67], v[144:147]\n"
    "buffer_load_dwordx4 v230, s[24:27], s92 offen offset:2048 nt lds\n"
    "v_mfma_f32_16x16x32_bf16 v[144:147], a[100:103], v[72:75], v[144:147]\n"
    "ds_read_b64_tr_b16 a[162:163], v250 offset:18432\n"
    "v_rcp_f32 v233, v252\n"
    "v_mfma_f32_16x16x32_bf16 v[144:147], a[104:107], v[80:83], v[144:147]\n"
    "ds_read_b64_tr_b16 a[164:165], v250 offset:20480\n"
    "v_mfma_f32_16x16x32_bf16 v[144:147], a[108:111], v[88:91], v[144:147]\n"
    "ds_read_b64_tr_b16 a[166:167], v250 offset:22528\n"
    "v_mfma_f32_16x16x32_bf16 v[148:151], a[96:99], v[68:71], v[148:151]\n"
    "ds_read_b64_tr_b16 a[168:169], v250 offset:24576\n"
    "v_mfma_f32_16x16x32_bf16 v[148:151], a[100:103], v[76:79], v[148:151]\n"
    "ds_read_b64_tr_b16 a[170:171], v250 offset:26624\n"
    "v_mfma_f32_16x16x32_bf16 v[148:151], a[104:107], v[84:87], v[148:151]\n"
    "ds_read_b64_tr_b16 a[172:173], v250 offset:28672\n"
    "v_mfma_f32_16x16x32_bf16 v[148:151], a[108:111], v[92:95], v[148:151]\n"
    "ds_read_b64_tr_b16 a[174:175], v250 offset:30720\n"
    "v_min_f32 v208, v204, v252\n"
    "v_mfma_f32_16x16x32_bf16 v[152:155], a[112:115], v[64:67], v[152:155]\n"
    "buffer_load_dwordx4 v231, s[24:27], s92 offen offset:3072 nt lds\n"
    "v_mfma_f32_16x16x32_bf16 v[152:155], a[116:119], v[72:75], v[152:155]\n"
    "ds_read_b64_tr_b16 a[176:177], v251 offset:16384\n"
    "ds_read_b64_tr_b16 a[178:179], v251 offset:18432\n"
    "v_mfma_f32_16x16x32_bf16 v[152:155], a[120:123], v[80:83], v[152:155]\n"
    "ds_read_b64_tr_b16 a[180:181], v251 offset:20480\n"
    "v_mfma_f32_16x16x32_bf16 v[152:155], a[124:127], v[88:91], v[152:155]\n"
    "ds_read_b64_tr_b16 a[182:183], v251 offset:22528\n"
    "v_mfma_f32_16x16x32_bf16 v[156:159], a[112:115], v[68:71], v[156:159]\n"
    "ds_read_b64_tr_b16 a[184:185], v251 offset:24576\n"
    "v_mfma_f32_16x16x32_bf16 v[156:159], a[116:119], v[76:79], v[156:159]\n"
    "ds_read_b64_tr_b16 a[186:187], v251 offset:26624\n"
    "v_max_f32 v209, v204, v252\n"
    "v_mfma_f32_16x16x32_bf16 v[156:159], a[120:123], v[84:87], v[156:159]\n"
    "ds_read_b64_tr_b16 a[188:189], v251 offset:28672\n"
    "v_mfma_f32_16x16x32_bf16 v[156:159], a[124:127], v[92:95], v[156:159]\n"
    "ds_read_b64_tr_b16 a[190:191], v251 offset:30720\n"
    "s_waitcnt lgkmcnt(10)\n"
    "v_mfma_f32_16x16x32_bf16 v[128:131], a[128:131], v[96:99], v[128:131]\n"
    "ds_read_b64_tr_b16 a[64:65], v240 offset:0\n"
    "v_mfma_f32_16x16x32_bf16 v[128:131], a[132:135], v[104:107], v[128:131]\n"
    "ds_read_b64_tr_b16 a[66:67], v240 offset:2048\n"
    "v_mfma_f32_16x16x32_bf16 v[128:131], a[136:139], v[112:115], v[128:131]\n"
    "ds_read_b64_tr_b16 a[68:69], v240 offset:4096\n"
    "ds_read_b64_tr_b16 a[70:71], v240 offset:6144\n"
    "v_mfma_f32_16x16x32_bf16 v[128:131], a[140:143], v[120:123], v[128:131]\n"
    "ds_read_b64_tr_b16 a[72:73], v240 offset:8192\n"
    "v_mfma_f32_16x16x32_bf16 v[132:135], a[128:131], v[100:103], v[132:135]\n"
    "ds_read_b64_tr_b16 a[74:75], v240 offset:10240\n"
    "v_cmp_u_f32 s[42:43], v204, v252\n"
    "v_mfma_f32_16x16x32_bf16 v[132:135], a[132:135], v[108:111], v[132:135]\n"
    "ds_read_b64_tr_b16 a[76:77], v240 offset:12288\n"
    "v_mfma_f32_16x16x32_bf16 v[132:135], a[136:139], v[116:119], v[132:135]\n"
    "ds_read_b64_tr_b16 a[78:79], v240 offset:14336\n"
    "v_mfma_f32_16x16x32_bf16 v[132:135], a[140:143], v[124:127], v[132:135]\n"
    "ds_read_b64_tr_b16 a[80:81], v241 offset:0\n"
    "v_mfma_f32_16x16x32_bf16 v[136:139], a[144:147], v[96:99], v[136:139]\n"
    "ds_read_b64_tr_b16 a[82:83], v241 offset:2048\n"
    "ds_read_b64_tr_b16 a[84:85], v241 offset:4096\n"
    "v_mfma_f32_16x16x32_bf16 v[136:139], a[148:151], v[104:107], v[136:139]\n"
    "ds_read_b64_tr_b16 a[86:87], v241 offset:6144\n"
    "v_mfma_f32_16x16x32_bf16 v[136:139], a[152:155], v[112:115], v[136:139]\n"
    "ds_read_b64_tr_b16 a[88:89], v241 offset:8192\n"
    "v_cmp_nle_f32 vcc, 0x21800000, v208\n"
    "v_mfma_f32_16x16x32_bf16 v[136:139], a[156:159], v[120:123], v[136:139]\n"
    "ds_read_b64_tr_b16 a[90:91], v241 offset:10240\n"
    "v_mfma_f32_16x16x32_bf16 v[140:143], a[144:147], v[100:103], v[140:143]\n"
    "ds_read_b64_tr_b16 a[92:93], v241 offset:12288\n"
    "v_mfma_f32_16x16x32_bf16 v[140:143], a[148:151], v[108:111], v[140:143]\n"
    "ds_read_b64_tr_b16 a[94:95], v241 offset:14336\n"
    "ds_read_b64_tr_b16 a[96:97], v242 offset:0\n"
    "v_mfma_f32_16x16x32_bf16 v[140:143], a[152:155], v[116:119], v[140:143]\n"
    "ds_read_b64_tr_b16 a[98:99], v242 offset:2048\n"
    "v_mfma_f32_16x16x32_bf16 v[140:143], a[156:159], v[124:127], v[140:143]\n"
    "ds_read_b64_tr_b16 a[100:101], v242 offset:4096\n"
    "v_mfma_f32_16x16x32_bf16 v[144:147], a[160:163], v[96:99], v[144:147]\n"
    "ds_read_b64_tr_b16 a[102:103], v242 offset:6144\n"
    "s_or_b64 s[48:49], vcc, s[42:43]\n"
    "v_mfma_f32_16x16x32_bf16 v[144:147], a[164:167], v[104:107], v[144:147]\n"
    "ds_read_b64_tr_b16 a[104:105], v242 offset:8192\n"
    "v_mfma_f32_16x16x32_bf16 v[144:147], a[168:171], v[112:115], v[144:147]\n"
    "ds_read_b64_tr_b16 a[106:107], v242 offset:10240\n"
    "ds_read_b64_tr_b16 a[108:109], v242 offset:12288\n"
    "s_waitcnt lgkmcnt(12)\n"
    "v_mfma_f32_16x16x32_bf16 v[144:147], a[172:175], v[120:123], v[144:147]\n"
    "ds_read_b64_tr_b16 a[110:111], v242 offset:14336\n"
    "v_mfma_f32_16x16x32_bf16 v[148:151], a[160:163], v[100:103], v[148:151]\n"
    "ds_read_b64_tr_b16 a[112:113], v243 offset:0\n"
    "v_mfma_f32_16x16x32_bf16 v[148:151], a[164:167], v[108:111], v[148:151]\n"
    "ds_read_b64_tr_b16 a[114:115], v243 offset:2048\n"
    "v_mfma_f32_16x16x32_bf16 v[148:151], a[168:171], v[116:119], v[148:151]\n"
    "ds_read_b64_tr_b16 a[116:117], v243 offset:4096\n"
    "v_cmp_nge_f32 vcc, 0x5d800000, v209\n"
    "v_mfma_f32_16x16x32_bf16 v[148:151], a[172:175], v[124:127], v[148:151]\n"
    "ds_read_b64_tr_b16 a[118:119], v243 offset:6144\n"
    "v_mfma_f32_16x16x32_bf16 v[152:155], a[176:179], v[96:99], v[152:155]\n"
    "ds_read_b64_tr_b16 a[120:121], v243 offset:8192\n"
    "ds_read_b64_tr_b16 a[122:123], v243 offset:10240\n"
    "v_mfma_f32_16x16x32_bf16 v[152:155], a[180:183], v[104:107], v[152:155]\n"
    "ds_read_b64_tr_b16 a[124:125], v243 offset:12288\n"
    "v_mfma_f32_16x16x32_bf16 v[152:155], a[184:187], v[112:115], v[152:155]\n"
    "ds_read_b64_tr_b16 a[126:127], v243 offset:14336\n"
    "v_mfma_f32_16x16x32_bf16 v[152:155], a[188:191], v[120:123], v[152:155]\n"
    "v_mfma_f32_16x16x32_bf16 v[156:159], a[176:179], v[100:103], v[156:159]\n"
    "s_or_b64 s[48:49], vcc, s[48:49]\n"
    "v_mfma_f32_16x16x32_bf16 v[156:159], a[180:183], v[108:111], v[156:159]\n"
    "v_mfma_f32_16x16x32_bf16 v[156:159], a[184:187], v[116:119], v[156:159]\n"
    "v_mfma_f32_16x16x32_bf16 v[156:159], a[188:191], v[124:127], v[156:159]\n"
    "s_waitcnt vmcnt(24) lgkmcnt(0)\n"
    "s_barrier\n"
    "v_mfma_f32_16x16x32_bf16 v[160:163], a[64:67], v[0:3], 0\n"
    "ds_read_b64_tr_b16 a[128:129], v240 offset:16384\n"
    "ds_read_b64_tr_b16 a[130:131], v240 offset:18432\n"
    "v_mfma_f32_16x16x32_bf16 v[160:163], a[68:71], v[8:11], v[160:163]\n"
    "ds_read_b64_tr_b16 a[132:133], v240 offset:20480\n"
    "s_add_u32 m0, s33, 0x24000\n"
    "v_mfma_f32_16x16x32_bf16 v[160:163], a[72:75], v[16:19], v[160:163]\n"
    "buffer_load_dwordx4 v228, s[24:27], s93 offen offset:0 nt lds\n"
    "v_mul_f32 v128, v128, v216\n"
    "v_mfma_f32_16x16x32_bf16 v[160:163], a[76:79], v[24:27], v[160:163]\n"
    "ds_read_b64_tr_b16 a[134:135], v240 offset:22528\n"
    "v_mul_f32 v129, v129, v216\n"
    "v_mfma_f32_16x16x32_bf16 v[164:167], a[64:67], v[4:7], 0\n"
    "buffer_load_dwordx4 v229, s[24:27], s93 offen offset:1024 nt lds\n"
    "ds_read_b64_tr_b16 a[136:137], v240 offset:24576\n"
    "v_mul_f32 v130, v130, v216\n"
    "v_mfma_f32_16x16x32_bf16 v[164:167], a[68:71], v[12:15], v[164:167]\n"
    "ds_read_b64_tr_b16 a[138:139], v240 offset:26624\n"
    "v_mul_f32 v131, v131, v216\n"
    "v_mfma_f32_16x16x32_bf16 v[164:167], a[72:75], v[20:23], v[164:167]\n"
    "buffer_load_dwordx4 v230, s[24:27], s93 offen offset:2048 nt lds\n"
    "ds_read_b64_tr_b16 a[140:141], v240 offset:28672\n"
    "v_mfma_f32_16x16x32_bf16 v[164:167], a[76:79], v[28:31], v[164:167]\n"
    "ds_read_b64_tr_b16 a[142:143], v240 offset:30720\n"
    "v_mul_f32 v132, v132, v233\n"
    "v_mfma_f32_16x16x32_bf16 v[168:171], a[80:83], v[0:3], 0\n"
    "buffer_load_dwordx4 v231, s[24:27], s93 offen offset:3072 nt lds\n"
    "v_mul_f32 v133, v133, v233\n"
    "v_mfma_f32_16x16x32_bf16 v[168:171], a[84:87], v[8:11], v[168:171]\n"
    "ds_read_b64_tr_b16 a[144:145], v241 offset:16384\n"
    "s_add_u32 m0, s33, 0x0\n"
    "v_mfma_f32_16x16x32_bf16 v[168:171], a[88:91], v[16:19], v[168:171]\n"
    "buffer_load_dwordx4 v228, s[24:27], s94 offen offset:0 nt lds\n"
    "v_mul_f32 v134, v134, v233\n"
    "v_mfma_f32_16x16x32_bf16 v[168:171], a[92:95], v[24:27], v[168:171]\n"
    "ds_read_b64_tr_b16 a[146:147], v241 offset:18432\n"
    "v_mul_f32 v135, v135, v233\n"
    "v_mfma_f32_16x16x32_bf16 v[172:175], a[80:83], v[4:7], 0\n"
    "buffer_load_dwordx4 v229, s[24:27], s94 offen offset:1024 nt lds\n"
    "v_cvt_pk_bf16_f32 v192, v128, v129\n"
    "v_mfma_f32_16x16x32_bf16 v[172:175], a[84:87], v[12:15], v[172:175]\n"
    "ds_read_b64_tr_b16 a[148:149], v241 offset:20480\n"
    "v_cvt_pk_bf16_f32 v193, v130, v131\n"
    "v_mfma_f32_16x16x32_bf16 v[172:175], a[88:91], v[20:23], v[172:175]\n"
    "ds_read_b64_tr_b16 a[150:151], v241 offset:22528\n"
    "v_cvt_pk_bf16_f32 v194, v132, v133\n"
    "v_mfma_f32_16x16x32_bf16 v[172:175], a[92:95], v[28:31], v[172:175]\n"
    "ds_read_b64_tr_b16 a[152:153], v241 offset:24576\n"
    "v_cvt_pk_bf16_f32 v195, v134, v135\n"
    "v_mfma_f32_16x16x32_bf16 v[176:179], a[96:99], v[0:3], 0\n"
    "buffer_load_dwordx4 v230, s[24:27], s94 offen offset:2048 nt lds\n"
    "ds_read_b64_tr_b16 a[154:155], v241 offset:26624\n"
    "v_mfma_f32_16x16x32_bf16 v[176:179], a[100:103], v[8:11], v[176:179]\n"
    "ds_read_b64_tr_b16 a[156:157], v241 offset:28672\n"
    "v_mul_f32 v136, v136, v216\n"
    "v_mfma_f32_16x16x32_bf16 v[176:179], a[104:107], v[16:19], v[176:179]\n"
    "ds_read_b64_tr_b16 a[158:159], v241 offset:30720\n"
    "v_mul_f32 v137, v137, v216\n"
    "v_mfma_f32_16x16x32_bf16 v[176:179], a[108:111], v[24:27], v[176:179]\n"
    "ds_read_b64_tr_b16 a[160:161], v242 offset:16384\n"
    "v_permlane16_swap_b32 v192, v194\n"
    "v_mfma_f32_16x16x32_bf16 v[180:183], a[96:99], v[4:7], 0\n"
    "ds_read_b64_tr_b16 a[162:163], v242 offset:18432\n"
    "v_permlane16_swap_b32 v193, v195\n"
    "v_mfma_f32_16x16x32_bf16 v[180:183], a[100:103], v[12:15], v[180:183]\n"
    "ds_read_b64_tr_b16 a[164:165], v242 offset:20480\n"
    "v_mul_f32 v138, v138, v216\n"
    "v_mfma_f32_16x16x32_bf16 v[180:183], a[104:107], v[20:23], v[180:183]\n"
    "ds_read_b64_tr_b16 a[166:167], v242 offset:22528\n"
    "v_mul_f32 v139, v139, v216\n"
    "v_mfma_f32_16x16x32_bf16 v[180:183], a[108:111], v[28:31], v[180:183]\n"
    "ds_read_b64_tr_b16 a[168:169], v242 offset:24576\n"
    "v_mul_f32 v140, v140, v233\n"
    "v_mfma_f32_16x16x32_bf16 v[184:187], a[112:115], v[0:3], 0\n"
    "buffer_load_dwordx4 v231, s[24:27], s94 offen offset:3072 nt lds\n"
    "ds_read_b64_tr_b16 a[170:171], v242 offset:26624\n"
    "v_mfma_f32_16x16x32_bf16 v[184:187], a[116:119], v[8:11], v[184:187]\n"
    "ds_read_b64_tr_b16 a[172:173], v242 offset:28672\n"
    "v_mul_f32 v141, v141, v233\n"
    "v_mfma_f32_16x16x32_bf16 v[184:187], a[120:123], v[16:19], v[184:187]\n"
    "ds_read_b64_tr_b16 a[174:175], v242 offset:30720\n"
    "v_mul_f32 v142, v142, v233\n"
    "v_mfma_f32_16x16x32_bf16 v[184:187], a[124:127], v[24:27], v[184:187]\n"
    "ds_read_b64_tr_b16 a[176:177], v243 offset:16384\n"
    "v_mul_f32 v143, v143, v233\n"
    "v_mfma_f32_16x16x32_bf16 v[188:191], a[112:115], v[4:7], 0\n"
    "ds_read_b64_tr_b16 a[178:179], v243 offset:18432\n"
    "v_cvt_pk_bf16_f32 v196, v136, v137\n"
    "v_mfma_f32_16x16x32_bf16 v[188:191], a[116:119], v[12:15], v[188:191]\n"
    "ds_read_b64_tr_b16 a[180:181], v243 offset:20480\n"
    "ds_read_b64_tr_b16 a[182:183], v243 offset:22528\n"
    "v_mfma_f32_16x16x32_bf16 v[188:191], a[120:123], v[20:23], v[188:191]\n"
    "ds_read_b64_tr_b16 a[184:185], v243 offset:24576\n"
    "ds_read_b64_tr_b16 a[186:187], v243 offset:26624\n"
    "v_mfma_f32_16x16x32_bf16 v[188:191], a[124:127], v[28:31], v[188:191]\n"
    "ds_read_b64_tr_b16 a[188:189], v243 offset:28672\n"
    "ds_read_b64_tr_b16 a[190:191], v243 offset:30720\n"
    "s_waitcnt lgkmcnt(11)\n"
    "v_mfma_f32_16x16x32_bf16 v[160:163], a[128:131], v[32:35], v[160:163]\n"
    "ds_read_b64_tr_b16 a[64:65], v240 offset:32768\n"
    "v_cvt_pk_bf16_f32 v197, v138, v139\n"
    "v_mfma_f32_16x16x32_bf16 v[160:163], a[132:135], v[40:43], v[160:163]\n"
    "ds_read_b64_tr_b16 a[66:67], v240 offset:34816\n"
    "v_cvt_pk_bf16_f32 v198, v140, v141\n"
    "v_mfma_f32_16x16x32_bf16 v[160:163], a[136:139], v[48:51], v[160:163]\n"
    "ds_read_b64_tr_b16 a[68:69], v240 offset:36864\n"
    "v_cvt_pk_bf16_f32 v199, v142, v143\n"
    "v_mfma_f32_16x16x32_bf16 v[160:163], a[140:143], v[56:59], v[160:163]\n"
    "ds_read_b64_tr_b16 a[70:71], v240 offset:38912\n"
    "v_mul_f32 v144, v144, v216\n"
    "v_mfma_f32_16x16x32_bf16 v[164:167], a[128:131], v[36:39], v[164:167]\n"
    "ds_read_b64_tr_b16 a[72:73], v240 offset:40960\n"
    "v_mul_f32 v145, v145, v216\n"
    "v_mfma_f32_16x16x32_bf16 v[164:167], a[132:135], v[44:47], v[164:167]\n"
    "ds_read_b64_tr_b16 a[74:75], v240 offset:43008\n"
    "v_permlane16_swap_b32 v196, v198\n"
    "v_mfma_f32_16x16x32_bf16 v[164:167], a[136:139], v[52:55], v[164:167]\n"
    "ds_read_b64_tr_b16 a[76:77], v240 offset:45056\n"
    "v_permlane16_swap_b32 v197, v199\n"
    "v_mfma_f32_16x16x32_bf16 v[164:167], a[140:143], v[60:63], v[164:167]\n"
    "ds_read_b64_tr_b16 a[78:79], v240 offset:47104\n"
    "v_mul_f32 v146, v146, v216\n"
    "v_mfma_f32_16x16x32_bf16 v[168:171], a[144:147], v[32:35], v[168:171]\n"
    "ds_read_b64_tr_b16 a[80:81], v241 offset:32768\n"
    "v_mul_f32 v147, v147, v216\n"
    "v_mfma_f32_16x16x32_bf16 v[168:171], a[148:151], v[40:43], v[168:171]\n"
    "ds_read_b64_tr_b16 a[82:83], v241 offset:34816\n"
    "v_mul_f32 v148, v148, v233\n"
    "v_mfma_f32_16x16x32_bf16 v[168:171], a[152:155], v[48:51], v[168:171]\n"
    "ds_read_b64_tr_b16 a[84:85], v241 offset:36864\n"
    "v_mul_f32 v149, v149, v233\n"
    "v_mfma_f32_16x16x32_bf16 v[168:171], a[156:159], v[56:59], v[168:171]\n"
    "ds_read_b64_tr_b16 a[86:87], v241 offset:38912\n"
    "v_mul_f32 v150, v150, v233\n"
    "v_mfma_f32_16x16x32_bf16 v[172:175], a[144:147], v[36:39], v[172:175]\n"
    "ds_read_b64_tr_b16 a[88:89], v241 offset:40960\n"
    "v_mul_f32 v151, v151, v233\n"
    "v_mfma_f32_16x16x32_bf16 v[172:175], a[148:151], v[44:47], v[172:175]\n"
    "ds_read_b64_tr_b16 a[90:91], v241 offset:43008\n"
    "v_cvt_pk_bf16_f32 v200, v144, v145\n"
    "v_mfma_f32_16x16x32_bf16 v[172:175], a[152:155], v[52:55], v[172:175]\n"
    "ds_read_b64_tr_b16 a[92:93], v241 offset:45056\n"
    "v_cvt_pk_bf16_f32 v201, v146, v147\n"
    "v_mfma_f32_16x16x32_bf16 v[172:175], a[156:159], v[60:63], v[172:175]\n"
    "ds_read_b64_tr_b16 a[94:95], v241 offset:47104\n"
    "v_cvt_pk_bf16_f32 v202, v148, v149\n"
    "v_mfma_f32_16x16x32_bf16 v[176:179], a[160:163], v[32:35], v[176:179]\n"
    "ds_read_b64_tr_b16 a[96:97], v242 offset:32768\n"
    "v_cvt_pk_bf16_f32 v203, v150, v151\n"
    "v_mfma_f32_16x16x32_bf16 v[176:179], a[164:167], v[40:43], v[176:179]\n"
    "ds_read_b64_tr_b16 a[98:99], v242 offset:34816\n"
    "v_mul_f32 v152, v152, v216\n"
    "s_waitcnt lgkmcnt(8)\n"
    "v_mfma_f32_16x16x32_bf16 v[176:179], a[168:171], v[48:51], v[176:179]\n"
    "ds_read_b64_tr_b16 a[100:101], v242 offset:36864\n"
    "v_mul_f32 v153, v153, v216\n"
    "v_mfma_f32_16x16x32_bf16 v[176:179], a[172:175], v[56:59], v[176:179]\n"
    "ds_read_b64_tr_b16 a[102:103], v242 offset:38912\n"
    "v_permlane16_swap_b32 v200, v202\n"
    "v_mfma_f32_16x16x32_bf16 v[180:183], a[160:163], v[36:39], v[180:183]\n"
    "ds_read_b64_tr_b16 a[104:105], v242 offset:40960\n"
    "v_permlane16_swap_b32 v201, v203\n"
    "v_mfma_f32_16x16x32_bf16 v[180:183], a[164:167], v[44:47], v[180:183]\n"
    "ds_read_b64_tr_b16 a[106:107], v242 offset:43008\n"
    "v_mul_f32 v154, v154, v216\n"
    "v_mfma_f32_16x16x32_bf16 v[180:183], a[168:171], v[52:55], v[180:183]\n"
    "ds_read_b64_tr_b16 a[108:109], v242 offset:45056\n"
    "v_mul_f32 v155, v155, v216\n"
    "v_mfma_f32_16x16x32_bf16 v[180:183], a[172:175], v[60:63], v[180:183]\n"
    "ds_read_b64_tr_b16 a[110:111], v242 offset:47104\n"
    "v_mul_f32 v156, v156, v233\n"
    "v_mfma_f32_16x16x32_bf16 v[184:187], a[176:179], v[32:35], v[184:187]\n"
    "ds_read_b64_tr_b16 a[112:113], v243 offset:32768\n"
    "v_mul_f32 v157, v157, v233\n"
    "v_mfma_f32_16x16x32_bf16 v[184:187], a[180:183], v[40:43], v[184:187]\n"
    "ds_read_b64_tr_b16 a[114:115], v243 offset:34816\n"
    "v_mul_f32 v158, v158, v233\n"
    "v_mfma_f32_16x16x32_bf16 v[184:187], a[184:187], v[48:51], v[184:187]\n"
    "ds_read_b64_tr_b16 a[116:117], v243 offset:36864\n"
    "v_mul_f32 v159, v159, v233\n"
    "v_mfma_f32_16x16x32_bf16 v[184:187], a[188:191], v[56:59], v[184:187]\n"
    "ds_read_b64_tr_b16 a[118:119], v243 offset:38912\n"
    "v_cvt_pk_bf16_f32 v204, v152, v153\n"
    "v_mfma_f32_16x16x32_bf16 v[188:191], a[176:179], v[36:39], v[188:191]\n"
    "ds_read_b64_tr_b16 a[120:121], v243 offset:40960\n"
    "v_cvt_pk_bf16_f32 v205, v154, v155\n"
    "v_mfma_f32_16x16x32_bf16 v[188:191], a[180:183], v[44:47], v[188:191]\n"
    "ds_read_b64_tr_b16 a[122:123], v243 offset:43008\n"
    "v_cvt_pk_bf16_f32 v206, v156, v157\n"
    "v_mfma_f32_16x16x32_bf16 v[188:191], a[184:187], v[52:55], v[188:191]\n"
    "ds_read_b64_tr_b16 a[124:125], v243 offset:45056\n"
    "v_cvt_pk_bf16_f32 v207, v158, v159\n"
    "v_mfma_f32_16x16x32_bf16 v[188:191], a[188:191], v[60:63], v[188:191]\n"
    "ds_read_b64_tr_b16 a[126:127], v243 offset:47104\n"
    "s_waitcnt vmcnt(24) lgkmcnt(8)\n"
    "s_barrier\n"
    "v_mfma_f32_16x16x32_bf16 v[160:163], a[64:67], v[64:67], v[160:163]\n"
    "ds_read_b64_tr_b16 a[128:129], v240 offset:49152\n"
    "s_mov_b64 vcc, s[44:45]\n"
    "v_mfma_f32_16x16x32_bf16 v[160:163], a[68:71], v[72:75], v[160:163]\n"
    "ds_read_b64_tr_b16 a[130:131], v240 offset:51200\n"
    "s_add_u32 m0, s33, 0x4000\n"
    "v_mfma_f32_16x16x32_bf16 v[160:163], a[72:75], v[80:83], v[160:163]\n"
    "buffer_load_dwordx4 v228, s[24:27], s95 offen offset:0 nt lds\n"
    "v_mfma_f32_16x16x32_bf16 v[160:163], a[76:79], v[88:91], v[160:163]\n"
    "ds_read_b64_tr_b16 a[132:133], v240 offset:53248\n"
    "v_cndmask_b32_dpp v220, v196, v192, vcc quad_perm:[1,0,3,2] row_mask:0xf bank_mask:0xf\n"
    "v_mfma_f32_16x16x32_bf16 v[164:167], a[64:67], v[68:71], v[164:167]\n"
    "buffer_load_dwordx4 v229, s[24:27], s95 offen offset:1024 nt lds\n"
    "v_mfma_f32_16x16x32_bf16 v[164:167], a[68:71], v[76:79], v[164:167]\n"
    "ds_read_b64_tr_b16 a[134:135], v240 offset:55296\n"
    "v_cndmask_b32_dpp v221, v197, v193, vcc quad_perm:[1,0,3,2] row_mask:0xf bank_mask:0xf\n"
    "v_mfma_f32_16x16x32_bf16 v[164:167], a[72:75], v[84:87], v[164:167]\n"
    "buffer_load_dwordx4 v230, s[24:27], s95 offen offset:2048 nt lds\n"
    "v_mfma_f32_16x16x32_bf16 v[164:167], a[76:79], v[92:95], v[164:167]\n"
    "ds_read_b64_tr_b16 a[136:137], v240 offset:57344\n"
    "ds_read_b64_tr_b16 a[138:139], v240 offset:59392\n"
    "v_mfma_f32_16x16x32_bf16 v[168:171], a[80:83], v[64:67], v[168:171]\n"
    "buffer_load_dwordx4 v231, s[24:27], s95 offen offset:3072 nt lds\n"
    "v_mfma_f32_16x16x32_bf16 v[168:171], a[84:87], v[72:75], v[168:171]\n"
    "ds_read_b64_tr_b16 a[140:141], v240 offset:61440\n"
    "v_permlane16_swap_b32 v204, v206\n"
    "v_mfma_f32_16x16x32_bf16 v[168:171], a[88:91], v[80:83], v[168:171]\n"
    "ds_read_b64_tr_b16 a[142:143], v240 offset:63488\n"
    "v_permlane16_swap_b32 v205, v207\n"
    "v_mfma_f32_16x16x32_bf16 v[168:171], a[92:95], v[88:91], v[168:171]\n"
    "ds_read_b64_tr_b16 a[144:145], v241 offset:49152\n"
    "v_cndmask_b32_dpp v222, v198, v194, vcc quad_perm:[1,0,3,2] row_mask:0xf bank_mask:0xf\n"
    "v_mfma_f32_16x16x32_bf16 v[172:175], a[80:83], v[68:71], v[172:175]\n"
    "ds_read_b64_tr_b16 a[146:147], v241 offset:51200\n"
    "v_cndmask_b32_dpp v223, v199, v195, vcc quad_perm:[1,0,3,2] row_mask:0xf bank_mask:0xf\n"
    "v_mfma_f32_16x16x32_bf16 v[172:175], a[84:87], v[76:79], v[172:175]\n"
    "ds_read_b64_tr_b16 a[148:149], v241 offset:53248\n"
    "s_not_b64 vcc, vcc\n"
    "v_mfma_f32_16x16x32_bf16 v[172:175], a[88:91], v[84:87], v[172:175]\n"
    "ds_read_b64_tr_b16 a[150:151], v241 offset:55296\n"
    "v_cndmask_b32_dpp v196, v192, v196, vcc quad_perm:[1,0,3,2] row_mask:0xf bank_mask:0xf\n"
    "v_mfma_f32_16x16x32_bf16 v[172:175], a[92:95], v[92:95], v[172:175]\n"
    "ds_read_b64_tr_b16 a[152:153], v241 offset:57344\n"
    "v_cndmask_b32_dpp v197, v193, v197, vcc quad_perm:[1,0,3,2] row_mask:0xf bank_mask:0xf\n"
    "v_mfma_f32_16x16x32_bf16 v[176:179], a[96:99], v[64:67], v[176:179]\n"
    "ds_read_b64_tr_b16 a[154:155], v241 offset:59392\n"
    "ds_read_b64_tr_b16 a[156:157], v241 offset:61440\n"
    "v_mfma_f32_16x16x32_bf16 v[176:179], a[100:103], v[72:75], v[176:179]\n"
    "ds_read_b64_tr_b16 a[158:159], v241 offset:63488\n"
    "v_cndmask_b32_dpp v198, v194, v198, vcc quad_perm:[1,0,3,2] row_mask:0xf bank_mask:0xf\n"
    "v_mfma_f32_16x16x32_bf16 v[176:179], a[104:107], v[80:83], v[176:179]\n"
    "ds_read_b64_tr_b16 a[160:161], v242 offset:49152\n"
    "ds_read_b64_tr_b16 a[162:163], v242 offset:51200\n"
    "v_mfma_f32_16x16x32_bf16 v[176:179], a[108:111], v[88:91], v[176:179]\n"
    "ds_read_b64_tr_b16 a[164:165], v242 offset:53248\n"
    "v_cndmask_b32_dpp v199, v195, v199, vcc quad_perm:[1,0,3,2] row_mask:0xf bank_mask:0xf\n"
    "v_mfma_f32_16x16x32_bf16 v[180:183], a[96:99], v[68:71], v[180:183]\n"
    "ds_read_b64_tr_b16 a[166:167], v242 offset:55296\n"
    "s_mov_b64 vcc, s[44:45]\n"
    "v_mfma_f32_16x16x32_bf16 v[180:183], a[100:103], v[76:79], v[180:183]\n"
    "ds_read_b64_tr_b16 a[168:169], v242 offset:57344\n"
    "ds_read_b64_tr_b16 a[170:171], v242 offset:59392\n"
    "v_mfma_f32_16x16x32_bf16 v[180:183], a[104:107], v[84:87], v[180:183]\n"
    "ds_read_b64_tr_b16 a[172:173], v242 offset:61440\n"
    "v_cndmask_b32_dpp v224, v204, v200, vcc quad_perm:[1,0,3,2] row_mask:0xf bank_mask:0xf\n"
    "v_mfma_f32_16x16x32_bf16 v[180:183], a[108:111], v[92:95], v[180:183]\n"
    "ds_read_b64_tr_b16 a[174:175], v242 offset:63488\n"
    "v_cndmask_b32_dpp v225, v205, v201, vcc quad_perm:[1,0,3,2] row_mask:0xf bank_mask:0xf\n"
    "s_waitcnt lgkmcnt(11)\n"
    "v_mfma_f32_16x16x32_bf16 v[184:187], a[112:115], v[64:67], v[184:187]\n"
    "ds_read_b64_tr_b16 a[176:177], v243 offset:49152\n"
    "v_cndmask_b32_dpp v226, v206, v202, vcc quad_perm:[1,0,3,2] row_mask:0xf bank_mask:0xf\n"
    "v_mfma_f32_16x16x32_bf16 v[184:187], a[116:119], v[72:75], v[184:187]\n"
    "ds_read_b64_tr_b16 a[178:179], v243 offset:51200\n"
    "v_mfma_f32_16x16x32_bf16 v[184:187], a[120:123], v[80:83], v[184:187]\n"
    "ds_read_b64_tr_b16 a[180:181], v243 offset:53248\n"
    "v_cndmask_b32_dpp v227, v207, v203, vcc quad_perm:[1,0,3,2] row_mask:0xf bank_mask:0xf\n"
    "v_mfma_f32_16x16x32_bf16 v[184:187], a[124:127], v[88:91], v[184:187]\n"
    "ds_read_b64_tr_b16 a[182:183], v243 offset:55296\n"
    "v_mfma_f32_16x16x32_bf16 v[188:191], a[112:115], v[68:71], v[188:191]\n"
    "ds_read_b64_tr_b16 a[184:185], v243 offset:57344\n"
    "s_not_b64 vcc, vcc\n"
    "v_mfma_f32_16x16x32_bf16 v[188:191], a[116:119], v[76:79], v[188:191]\n"
    "ds_read_b64_tr_b16 a[186:187], v243 offset:59392\n"
    "v_cndmask_b32_dpp v204, v200, v204, vcc quad_perm:[1,0,3,2] row_mask:0xf bank_mask:0xf\n"
    "v_mfma_f32_16x16x32_bf16 v[188:191], a[120:123], v[84:87], v[188:191]\n"
    "ds_read_b64_tr_b16 a[188:189], v243 offset:61440\n"
    "v_mfma_f32_16x16x32_bf16 v[188:191], a[124:127], v[92:95], v[188:191]\n"
    "ds_read_b64_tr_b16 a[190:191], v243 offset:63488\n"
    "v_cndmask_b32_dpp v205, v201, v205, vcc quad_perm:[1,0,3,2] row_mask:0xf bank_mask:0xf\n"
    "v_mfma_f32_16x16x32_bf16 v[160:163], a[128:131], v[96:99], v[160:163]\n"
    "ds_read_b64_tr_b16 a[64:65], v244 offset:0\n"
    "v_cndmask_b32_dpp v206, v202, v206, vcc quad_perm:[1,0,3,2] row_mask:0xf bank_mask:0xf\n"
    "v_mfma_f32_16x16x32_bf16 v[160:163], a[132:135], v[104:107], v[160:163]\n"
    "ds_read_b64_tr_b16 a[66:67], v244 offset:2048\n"
    "v_mfma_f32_16x16x32_bf16 v[160:163], a[136:139], v[112:115], v[160:163]\n"
    "ds_read_b64_tr_b16 a[68:69], v244 offset:4096\n"
    "v_cndmask_b32_dpp v207, v203, v207, vcc quad_perm:[1,0,3,2] row_mask:0xf bank_mask:0xf\n"
    "v_mfma_f32_16x16x32_bf16 v[160:163], a[140:143], v[120:123], v[160:163]\n"
    "ds_read_b64_tr_b16 a[70:71], v244 offset:6144\n"
    "v_mfma_f32_16x16x32_bf16 v[164:167], a[128:131], v[100:103], v[164:167]\n"
    "ds_read_b64_tr_b16 a[72:73], v244 offset:8192\n"
    "s_mov_b64 vcc, s[46:47]\n"
    "v_mfma_f32_16x16x32_bf16 v[164:167], a[132:135], v[108:111], v[164:167]\n"
    "ds_read_b64_tr_b16 a[74:75], v244 offset:10240\n"
    "v_cndmask_b32_dpp v192, v224, v220, vcc quad_perm:[2,3,0,1] row_mask:0xf bank_mask:0xf\n"
    "v_mfma_f32_16x16x32_bf16 v[164:167], a[136:139], v[116:119], v[164:167]\n"
    "ds_read_b64_tr_b16 a[76:77], v244 offset:12288\n"
    "v_mfma_f32_16x16x32_bf16 v[164:167], a[140:143], v[124:127], v[164:167]\n"
    "ds_read_b64_tr_b16 a[78:79], v244 offset:14336\n"
    "v_cndmask_b32_dpp v193, v225, v221, vcc quad_perm:[2,3,0,1] row_mask:0xf bank_mask:0xf\n"
    "v_mfma_f32_16x16x32_bf16 v[168:171], a[144:147], v[96:99], v[168:171]\n"
    "ds_read_b64_tr_b16 a[80:81], v245 offset:0\n"
    "v_cndmask_b32_dpp v194, v226, v222, vcc quad_perm:[2,3,0,1] row_mask:0xf bank_mask:0xf\n"
    "v_mfma_f32_16x16x32_bf16 v[168:171], a[148:151], v[104:107], v[168:171]\n"
    "ds_read_b64_tr_b16 a[82:83], v245 offset:2048\n"
    "s_waitcnt lgkmcnt(9)\n"
    "v_mfma_f32_16x16x32_bf16 v[168:171], a[152:155], v[112:115], v[168:171]\n"
    "ds_read_b64_tr_b16 a[84:85], v245 offset:4096\n"
    "v_cndmask_b32_dpp v195, v227, v223, vcc quad_perm:[2,3,0,1] row_mask:0xf bank_mask:0xf\n"
    "v_mfma_f32_16x16x32_bf16 v[168:171], a[156:159], v[120:123], v[168:171]\n"
    "ds_read_b64_tr_b16 a[86:87], v245 offset:6144\n"
    "v_mfma_f32_16x16x32_bf16 v[172:175], a[144:147], v[100:103], v[172:175]\n"
    "ds_read_b64_tr_b16 a[88:89], v245 offset:8192\n"
    "v_cndmask_b32_dpp v252, v204, v196, vcc quad_perm:[2,3,0,1] row_mask:0xf bank_mask:0xf\n"
    "v_mfma_f32_16x16x32_bf16 v[172:175], a[148:151], v[108:111], v[172:175]\n"
    "ds_read_b64_tr_b16 a[90:91], v245 offset:10240\n"
    "v_cndmask_b32_dpp v253, v205, v197, vcc quad_perm:[2,3,0,1] row_mask:0xf bank_mask:0xf\n"
    "v_mfma_f32_16x16x32_bf16 v[172:175], a[152:155], v[116:119], v[172:175]\n"
    "ds_read_b64_tr_b16 a[92:93], v245 offset:12288\n"
    "v_mfma_f32_16x16x32_bf16 v[172:175], a[156:159], v[124:127], v[172:175]\n"
    "ds_read_b64_tr_b16 a[94:95], v245 offset:14336\n"
    "v_cndmask_b32_dpp v254, v206, v198, vcc quad_perm:[2,3,0,1] row_mask:0xf bank_mask:0xf\n"
    "v_mfma_f32_16x16x32_bf16 v[176:179], a[160:163], v[96:99], v[176:179]\n"
    "ds_read_b64_tr_b16 a[96:97], v246 offset:0\n"
    "v_cndmask_b32_dpp v255, v207, v199, vcc quad_perm:[2,3,0,1] row_mask:0xf bank_mask:0xf\n"
    "v_mfma_f32_16x16x32_bf16 v[176:179], a[164:167], v[104:107], v[176:179]\n"
    "ds_read_b64_tr_b16 a[98:99], v246 offset:2048\n"
    "v_mfma_f32_16x16x32_bf16 v[176:179], a[168:171], v[112:115], v[176:179]\n"
    "ds_read_b64_tr_b16 a[100:101], v246 offset:4096\n"
    "s_not_b64 vcc, vcc\n"
    "v_mfma_f32_16x16x32_bf16 v[176:179], a[172:175], v[120:123], v[176:179]\n"
    "ds_read_b64_tr_b16 a[102:103], v246 offset:6144\n"
    "v_mfma_f32_16x16x32_bf16 v[180:183], a[160:163], v[100:103], v[180:183]\n"
    "ds_read_b64_tr_b16 a[104:105], v246 offset:8192\n"
    "v_cndmask_b32_dpp v200, v220, v224, vcc quad_perm:[2,3,0,1] row_mask:0xf bank_mask:0xf\n"
    "v_mfma_f32_16x16x32_bf16 v[180:183], a[164:167], v[108:111], v[180:183]\n"
    "ds_read_b64_tr_b16 a[106:107], v246 offset:10240\n"
    "v_cndmask_b32_dpp v201, v221, v225, vcc quad_perm:[2,3,0,1] row_mask:0xf bank_mask:0xf\n"
    "v_mfma_f32_16x16x32_bf16 v[180:183], a[168:171], v[116:119], v[180:183]\n"
    "ds_read_b64_tr_b16 a[108:109], v246 offset:12288\n"
    "v_mfma_f32_16x16x32_bf16 v[180:183], a[172:175], v[124:127], v[180:183]\n"
    "ds_read_b64_tr_b16 a[110:111], v246 offset:14336\n"
    "v_cndmask_b32_dpp v202, v222, v226, vcc quad_perm:[2,3,0,1] row_mask:0xf bank_mask:0xf\n"
    "v_mfma_f32_16x16x32_bf16 v[184:187], a[176:179], v[96:99], v[184:187]\n"
    "ds_read_b64_tr_b16 a[112:113], v247 offset:0\n"
    "v_cndmask_b32_dpp v203, v223, v227, vcc quad_perm:[2,3,0,1] row_mask:0xf bank_mask:0xf\n"
    "v_mfma_f32_16x16x32_bf16 v[184:187], a[180:183], v[104:107], v[184:187]\n"
    "ds_read_b64_tr_b16 a[114:115], v247 offset:2048\n"
    "v_mfma_f32_16x16x32_bf16 v[184:187], a[184:187], v[112:115], v[184:187]\n"
    "ds_read_b64_tr_b16 a[116:117], v247 offset:4096\n"
    "v_cndmask_b32_dpp v204, v196, v204, vcc quad_perm:[2,3,0,1] row_mask:0xf bank_mask:0xf\n"
    "v_mfma_f32_16x16x32_bf16 v[184:187], a[188:191], v[120:123], v[184:187]\n"
    "ds_read_b64_tr_b16 a[118:119], v247 offset:6144\n"
    "v_mfma_f32_16x16x32_bf16 v[188:191], a[176:179], v[100:103], v[188:191]\n"
    "ds_read_b64_tr_b16 a[120:121], v247 offset:8192\n"
    "v_cndmask_b32_dpp v205, v197, v205, vcc quad_perm:[2,3,0,1] row_mask:0xf bank_mask:0xf\n"
    "v_mfma_f32_16x16x32_bf16 v[188:191], a[180:183], v[108:111], v[188:191]\n"
    "ds_read_b64_tr_b16 a[122:123], v247 offset:10240\n"
    "v_cndmask_b32_dpp v206, v198, v206, vcc quad_perm:[2,3,0,1] row_mask:0xf bank_mask:0xf\n"
    "v_mfma_f32_16x16x32_bf16 v[188:191], a[184:187], v[116:119], v[188:191]\n"
    "ds_read_b64_tr_b16 a[124:125], v247 offset:12288\n"
    "v_cndmask_b32_dpp v207, v199, v207, vcc quad_perm:[2,3,0,1] row_mask:0xf bank_mask:0xf\n"
    "v_mfma_f32_16x16x32_bf16 v[188:191], a[188:191], v[124:127], v[188:191]\n"
    "ds_read_b64_tr_b16 a[126:127], v247 offset:14336\n"
    "s_waitcnt vmcnt(20) lgkmcnt(9)\n"
    "s_barrier\n"
    "v_mfma_f32_16x16x32_bf16 v[128:131], a[64:67], v[0:3], 0\n"
    "ds_read_b64_tr_b16 a[128:129], v244 offset:16384\n"
    "v_mfma_f32_16x16x32_bf16 v[128:131], a[68:71], v[8:11], v[128:131]\n"
    "ds_read_b64_tr_b16 a[130:131], v244 offset:18432\n"
    "v_mfma_f32_16x16x32_bf16 v[128:131], a[72:75], v[16:19], v[128:131]\n"
    "ds_read_b64_tr_b16 a[132:133], v244 offset:20480\n"
    "v_mul_f32 v160, v160, v216\n"
    "v_mfma_f32_16x16x32_bf16 v[128:131], a[76:79], v[24:27], v[128:131]\n"
    "ds_read_b64_tr_b16 a[134:135], v244 offset:22528\n"
    "v_mfma_f32_16x16x32_bf16 v[132:135], a[64:67], v[4:7], 0\n"
    "ds_read_b64_tr_b16 a[136:137], v244 offset:24576\n"
    "v_mul_f32 v161, v161, v216\n"
    "v_mfma_f32_16x16x32_bf16 v[132:135], a[68:71], v[12:15], v[132:135]\n"
    "ds_read_b64_tr_b16 a[138:139], v244 offset:26624\n"
    "v_mul_f32 v162, v162, v216\n"
    "v_mfma_f32_16x16x32_bf16 v[132:135], a[72:75], v[20:23], v[132:135]\n"
    "ds_read_b64_tr_b16 a[140:141], v244 offset:28672\n"
    "v_mfma_f32_16x16x32_bf16 v[132:135], a[76:79], v[28:31], v[132:135]\n"
    "ds_read_b64_tr_b16 a[142:143], v244 offset:30720\n"
    "v_mul_f32 v163, v163, v216\n"
    "v_mfma_f32_16x16x32_bf16 v[136:139], a[80:83], v[0:3], 0\n"
    "ds_read_b64_tr_b16 a[144:145], v245 offset:16384\n"
    "v_mul_f32 v164, v164, v233\n"
    "v_mfma_f32_16x16x32_bf16 v[136:139], a[84:87], v[8:11], v[136:139]\n"
    "ds_read_b64_tr_b16 a[146:147], v245 offset:18432\n"
    "v_mfma_f32_16x16x32_bf16 v[136:139], a[88:91], v[16:19], v[136:139]\n"
    "ds_read_b64_tr_b16 a[148:149], v245 offset:20480\n"
    "v_mul_f32 v165, v165, v233\n"
    "v_mfma_f32_16x16x32_bf16 v[136:139], a[92:95], v[24:27], v[136:139]\n"
    "ds_read_b64_tr_b16 a[150:151], v245 offset:22528\n"
    "v_mul_f32 v166, v166, v233\n"
    "v_mfma_f32_16x16x32_bf16 v[140:143], a[80:83], v[4:7], 0\n"
    "ds_read_b64_tr_b16 a[152:153], v245 offset:24576\n"
    "v_mfma_f32_16x16x32_bf16 v[140:143], a[84:87], v[12:15], v[140:143]\n"
    "ds_read_b64_tr_b16 a[154:155], v245 offset:26624\n"
    "v_mul_f32 v167, v167, v233\n"
    "v_mfma_f32_16x16x32_bf16 v[140:143], a[88:91], v[20:23], v[140:143]\n"
    "ds_read_b64_tr_b16 a[156:157], v245 offset:28672\n"
    "v_cvt_pk_bf16_f32 v196, v160, v161\n"
    "v_mfma_f32_16x16x32_bf16 v[140:143], a[92:95], v[28:31], v[140:143]\n"
    "ds_read_b64_tr_b16 a[158:159], v245 offset:30720\n"
    "v_mfma_f32_16x16x32_bf16 v[144:147], a[96:99], v[0:3], 0\n"
    "ds_read_b64_tr_b16 a[160:161], v246 offset:16384\n"
    "v_cvt_pk_bf16_f32 v197, v162, v163\n"
    "v_mfma_f32_16x16x32_bf16 v[144:147], a[100:103], v[8:11], v[144:147]\n"
    "ds_read_b64_tr_b16 a[162:163], v246 offset:18432\n"
    "v_cvt_pk_bf16_f32 v198, v164, v165\n"
    "v_mfma_f32_16x16x32_bf16 v[144:147], a[104:107], v[16:19], v[144:147]\n"
    "ds_read_b64_tr_b16 a[164:165], v246 offset:20480\n"
    "s_waitcnt lgkmcnt(9)\n"
    "v_mfma_f32_16x16x32_bf16 v[144:147], a[108:111], v[24:27], v[144:147]\n"
    "PAD4_%=:\ns_cbranch_execz PADRET_%=\n"
    "ds_read_b64_tr_b16 a[166:167], v246 offset:22528\n"
    "v_cvt_pk_bf16_f32 v199, v166, v167\n"
    "v_mfma_f32_16x16x32_bf16 v[148:151], a[96:99], v[4:7], 0\n"
    "ds_read_b64_tr_b16 a[168:169], v246 offset:24576\n"
    "v_mul_f32 v168, v168, v216\n"
    "v_mfma_f32_16x16x32_bf16 v[148:151], a[100:103], v[12:15], v[148:151]\n"
    "ds_read_b64_tr_b16 a[170:171], v246 offset:26624\n"
    "v_mfma_f32_16x16x32_bf16 v[148:151], a[104:107], v[20:23], v[148:151]\n"
    "ds_read_b64_tr_b16 a[172:173], v246 offset:28672\n"
    "v_mul_f32 v169, v169, v216\n"
    "v_mfma_f32_16x16x32_bf16 v[148:151], a[108:111], v[28:31], v[148:151]\n"
    "ds_read_b64_tr_b16 a[174:175], v246 offset:30720\n"
    "v_permlane16_swap_b32 v196, v198\n"
    "v_mfma_f32_16x16x32_bf16 v[152:155], a[112:115], v[0:3], 0\n"
    "ds_read_b64_tr_b16 a[176:177], v247 offset:16384\n"
    "v_permlane16_swap_b32 v197, v199\n"
    "v_mfma_f32_16x16x32_bf16 v[152:155], a[116:119], v[8:11], v[152:155]\n"
    "ds_read_b64_tr_b16 a[178:179], v247 offset:18432\n"
    "v_mfma_f32_16x16x32_bf16 v[152:155], a[120:123], v[16:19], v[152:155]\n"
    "ds_read_b64_tr_b16 a[180:181], v247 offset:20480\n"
    "v_mul_f32 v170, v170, v216\n"
    "v_mfma_f32_16x16x32_bf16 v[152:155], a[124:127], v[24:27], v[152:155]\n"
    "ds_read_b64_tr_b16 a[182:183], v247 offset:22528\n"
    "v_mul_f32 v171, v171, v216\n"
    "v_mfma_f32_16x16x32_bf16 v[156:159], a[112:115], v[4:7], 0\n"
    "ds_read_b64_tr_b16 a[184:185], v247 offset:24576\n"
    "v_mfma_f32_16x16x32_bf16 v[156:159], a[116:119], v[12:15], v[156:159]\n"
    "ds_read_b64_tr_b16 a[186:187], v247 offset:26624\n"
    "v_mul_f32 v172, v172, v233\n"
    "v_mfma_f32_16x16x32_bf16 v[156:159], a[120:123], v[20:23], v[156:159]\n"
    "ds_read_b64_tr_b16 a[188:189], v247 offset:28672\n"
    "v_mul_f32 v173, v173, v233\n"
    "v_mfma_f32_16x16x32_bf16 v[156:159], a[124:127], v[28:31], v[156:159]\n"
    "ds_read_b64_tr_b16 a[190:191], v247 offset:30720\n"
    "v_mfma_f32_16x16x32_bf16 v[128:131], a[128:131], v[32:35], v[128:131]\n"
    "ds_read_b64_tr_b16 a[64:65], v244 offset:32768\n"
    "v_mul_f32 v174, v174, v233\n"
    "v_mfma_f32_16x16x32_bf16 v[128:131], a[132:135], v[40:43], v[128:131]\n"
    "ds_read_b64_tr_b16 a[66:67], v244 offset:34816\n"
    "v_mul_f32 v175, v175, v233\n"
    "v_mfma_f32_16x16x32_bf16 v[128:131], a[136:139], v[48:51], v[128:131]\n"
    "ds_read_b64_tr_b16 a[68:69], v244 offset:36864\n"
    "v_mfma_f32_16x16x32_bf16 v[128:131], a[140:143], v[56:59], v[128:131]\n"
    "ds_read_b64_tr_b16 a[70:71], v244 offset:38912\n"
    "v_cvt_pk_bf16_f32 v220, v168, v169\n"
    "v_mfma_f32_16x16x32_bf16 v[132:135], a[128:131], v[36:39], v[132:135]\n"
    "ds_read_b64_tr_b16 a[72:73], v244 offset:40960\n"
    "v_cvt_pk_bf16_f32 v221, v170, v171\n"
    "v_mfma_f32_16x16x32_bf16 v[132:135], a[132:135], v[44:47], v[132:135]\n"
    "ds_read_b64_tr_b16 a[74:75], v244 offset:43008\n"
    "v_mfma_f32_16x16x32_bf16 v[132:135], a[136:139], v[52:55], v[132:135]\n"
    "ds_read_b64_tr_b16 a[76:77], v244 offset:45056\n"
    "v_cvt_pk_bf16_f32 v222, v172, v173\n"
    "v_mfma_f32_16x16x32_bf16 v[132:135], a[140:143], v[60:63], v[132:135]\n"
    "ds_read_b64_tr_b16 a[78:79], v244 offset:47104\n"
    "v_cvt_pk_bf16_f32 v223, v174, v175\n"
    "v_mfma_f32_16x16x32_bf16 v[136:139], a[144:147], v[32:35], v[136:139]\n"
    "ds_read_b64_tr_b16 a[80:81], v245 offset:32768\n"
    "s_waitcnt lgkmcnt(9)\n"
    "v_mfma_f32_16x16x32_bf16 v[136:139], a[148:151], v[40:43], v[136:139]\n"
    "ds_read_b64_tr_b16 a[82:83], v245 offset:34816\n"
    "v_mul_f32 v176, v176, v216\n"
    "v_mfma_f32_16x16x32_bf16 v[136:139], a[152:155], v[48:51], v[136:139]\n"
    "ds_read_b64_tr_b16 a[84:85], v245 offset:36864\n"
    "v_mul_f32 v177, v177, v216\n"
    "v_mfma_f32_16x16x32_bf16 v[136:139], a[156:159], v[56:59], v[136:139]\n"
    "ds_read_b64_tr_b16 a[86:87], v245 offset:38912\n"
    "v_mfma_f32_16x16x32_bf16 v[140:143], a[144:147], v[36:39], v[140:143]\n"
    "ds_read_b64_tr_b16 a[88:89], v245 offset:40960\n"
    "v_permlane16_swap_b32 v220, v222\n"
    "v_mfma_f32_16x16x32_bf16 v[140:143], a[148:151], v[44:47], v[140:143]\n"
    "ds_read_b64_tr_b16 a[90:91], v245 offset:43008\n"
    "v_permlane16_swap_b32 v221, v223\n"
    "v_mfma_f32_16x16x32_bf16 v[140:143], a[152:155], v[52:55], v[140:143]\n"
    "ds_read_b64_tr_b16 a[92:93], v245 offset:45056\n"
    "v_mfma_f32_16x16x32_bf16 v[140:143], a[156:159], v[60:63], v[140:143]\n"
    "ds_read_b64_tr_b16 a[94:95], v245 offset:47104\n"
    "v_mul_f32 v178, v178, v216\n"
    "v_mfma_f32_16x16x32_bf16 v[144:147], a[160:163], v[32:35], v[144:147]\n"
    "ds_read_b64_tr_b16 a[96:97], v246 offset:32768\n"
    "v_mul_f32 v179, v179, v216\n"
    "v_mfma_f32_16x16x32_bf16 v[144:147], a[164:167], v[40:43], v[144:147]\n"
    "ds_read_b64_tr_b16 a[98:99], v246 offset:34816\n"
    "v_mfma_f32_16x16x32_bf16 v[144:147], a[168:171], v[48:51], v[144:147]\n"
    "ds_read_b64_tr_b16 a[100:101], v246 offset:36864\n"
    "v_mul_f32 v180, v180, v233\n"
    "v_mfma_f32_16x16x32_bf16 v[144:147], a[172:175], v[56:59], v[144:147]\n"
    "ds_read_b64_tr_b16 a[102:103], v246 offset:38912\n"
    "v_mul_f32 v181, v181, v233\n"
    "v_mfma_f32_16x16x32_bf16 v[148:151], a[160:163], v[36:39], v[148:151]\n"
    "ds_read_b64_tr_b16 a[104:105], v246 offset:40960\n"
    "v_mfma_f32_16x16x32_bf16 v[148:151], a[164:167], v[44:47], v[148:151]\n"
    "ds_read_b64_tr_b16 a[106:107], v246 offset:43008\n"
    "v_mul_f32 v182, v182, v233\n"
    "v_mfma_f32_16x16x32_bf16 v[148:151], a[168:171], v[52:55], v[148:151]\n"
    "ds_read_b64_tr_b16 a[108:109], v246 offset:45056\n"
    "v_mul_f32 v183, v183, v233\n"
    "v_mfma_f32_16x16x32_bf16 v[148:151], a[172:175], v[60:63], v[148:151]\n"
    "ds_read_b64_tr_b16 a[110:111], v246 offset:47104\n"
    "v_mfma_f32_16x16x32_bf16 v[152:155], a[176:179], v[32:35], v[152:155]\n"
    "ds_read_b64_tr_b16 a[112:113], v247 offset:32768\n"
    "v_cvt_pk_bf16_f32 v224, v176, v177\n"
    "v_mfma_f32_16x16x32_bf16 v[152:155], a[180:183], v[40:43], v[152:155]\n"
    "ds_read_b64_tr_b16 a[114:115], v247 offset:34816\n"
    "v_cvt_pk_bf16_f32 v225, v178, v179\n"
    "v_mfma_f32_16x16x32_bf16 v[152:155], a[184:187], v[48:51], v[152:155]\n"
    "ds_read_b64_tr_b16 a[116:117], v247 offset:36864\n"
    "v_mfma_f32_16x16x32_bf16 v[152:155], a[188:191], v[56:59], v[152:155]\n"
    "ds_read_b64_tr_b16 a[118:119], v247 offset:38912\n"
    "v_cvt_pk_bf16_f32 v226, v180, v181\n"
    "v_mfma_f32_16x16x32_bf16 v[156:159], a[176:179], v[36:39], v[156:159]\n"
    "ds_read_b64_tr_b16 a[120:121], v247 offset:40960\n"
    "v_cvt_pk_bf16_f32 v227, v182, v183\n"
    "v_mfma_f32_16x16x32_bf16 v[156:159], a[180:183], v[44:47], v[156:159]\n"
    "ds_read_b64_tr_b16 a[122:123], v247 offset:43008\n"
    "v_mul_f32 v184, v184, v216\n"
    "v_mfma_f32_16x16x32_bf16 v[156:159], a[184:187], v[52:55], v[156:159]\n"
    "ds_read_b64_tr_b16 a[124:125], v247 offset:45056\n"
    "v_mul_f32 v185, v185, v216\n"
    "v_mfma_f32_16x16x32_bf16 v[156:159], a[188:191], v[60:63], v[156:159]\n"
    "ds_read_b64_tr_b16 a[126:127], v247 offset:47104\n"
    "s_waitcnt vmcnt(12) lgkmcnt(9)\n"
    "s_barrier\n"
    "v_mfma_f32_16x16x32_bf16 v[128:131], a[64:67], v[64:67], v[128:131]\n"
    "buffer_store_dwordx4 v[192:195], v232, s[28:31], 0 offen offset:0 nt\n"
    "v_mfma_f32_16x16x32_bf16 v[128:131], a[68:71], v[72:75], v[128:131]\n"
    "ds_read_b64_tr_b16 a[128:129], v244 offset:49152\n"
    "ds_read_b64_tr_b16 a[130:131], v244 offset:51200\n"
    "v_mfma_f32_16x16x32_bf16 v[128:131], a[72:75], v[80:83], v[128:131]\n"
    "ds_read_b64_tr_b16 a[132:133], v244 offset:53248\n"
    "v_permlane16_swap_b32 v224, v226\n"
    "v_mfma_f32_16x16x32_bf16 v[128:131], a[76:79], v[88:91], v[128:131]\n"
    "ds_read_b64_tr_b16 a[134:135], v244 offset:55296\n"
    "v_permlane16_swap_b32 v225, v227\n"
    "v_mfma_f32_16x16x32_bf16 v[132:135], a[64:67], v[68:71], v[132:135]\n"
    "ds_read_b64_tr_b16 a[136:137], v244 offset:57344\n"
    "v_mul_f32 v186, v186, v216\n"
    "v_mfma_f32_16x16x32_bf16 v[132:135], a[68:71], v[76:79], v[132:135]\n"
    "ds_read_b64_tr_b16 a[138:139], v244 offset:59392\n"
    "v_mul_f32 v187, v187, v216\n"
    "v_mfma_f32_16x16x32_bf16 v[132:135], a[72:75], v[84:87], v[132:135]\n"
    "ds_read_b64_tr_b16 a[140:141], v244 offset:61440\n"
    "v_mul_f32 v188, v188, v233\n"
    "v_mfma_f32_16x16x32_bf16 v[132:135], a[76:79], v[92:95], v[132:135]\n"
    "ds_read_b64_tr_b16 a[142:143], v244 offset:63488\n"
    "v_mul_f32 v189, v189, v233\n"
    "v_mfma_f32_16x16x32_bf16 v[136:139], a[80:83], v[64:67], v[136:139]\n"
    "buffer_store_dwordx4 v[252:255], v232, s[28:31], 0 offen offset:512 nt\n"
    "v_mfma_f32_16x16x32_bf16 v[136:139], a[84:87], v[72:75], v[136:139]\n"
    "ds_read_b64_tr_b16 a[144:145], v245 offset:49152\n"
    "ds_read_b64_tr_b16 a[146:147], v245 offset:51200\n"
    "v_mfma_f32_16x16x32_bf16 v[136:139], a[88:91], v[80:83], v[136:139]\n"
    "ds_read_b64_tr_b16 a[148:149], v245 offset:53248\n"
    "v_mul_f32 v190, v190, v233\n"
    "v_mfma_f32_16x16x32_bf16 v[136:139], a[92:95], v[88:91], v[136:139]\n"
    "ds_read_b64_tr_b16 a[150:151], v245 offset:55296\n"
    "v_mul_f32 v191, v191, v233\n"
    "v_mfma_f32_16x16x32_bf16 v[140:143], a[80:83], v[68:71], v[140:143]\n"
    "ds_read_b64_tr_b16 a[152:153], v245 offset:57344\n"
    "v_cvt_pk_bf16_f32 v236, v184, v185\n"
    "v_mfma_f32_16x16x32_bf16 v[140:143], a[84:87], v[76:79], v[140:143]\n"
    "ds_read_b64_tr_b16 a[154:155], v245 offset:59392\n"
    "v_mfma_f32_16x16x32_bf16 v[140:143], a[88:91], v[84:87], v[140:143]\n"
    "ds_read_b64_tr_b16 a[156:157], v245 offset:61440\n"
    "v_cvt_pk_bf16_f32 v237, v186, v187\n"
    "v_mfma_f32_16x16x32_bf16 v[140:143], a[92:95], v[92:95], v[140:143]\n"
    "ds_read_b64_tr_b16 a[158:159], v245 offset:63488\n"
    "v_cvt_pk_bf16_f32 v238, v188, v189\n"
    "v_mfma_f32_16x16x32_bf16 v[144:147], a[96:99], v[64:67], v[144:147]\n"
    "ds_read_b64_tr_b16 a[160:161], v246 offset:49152\n"
    "v_cvt_pk_bf16_f32 v239, v190, v191\n"
    "v_mfma_f32_16x16x32_bf16 v[144:147], a[100:103], v[72:75], v[144:147]\n"
    "ds_read_b64_tr_b16 a[162:163], v246 offset:51200\n"
    "ds_read_b64_tr_b16 a[164:165], v246 offset:53248\n"
    "v_mfma_f32_16x16x32_bf16 v[144:147], a[104:107], v[80:83], v[144:147]\n"
    "buffer_store_dwordx4 v[200:203], v232, s[28:31], 0 offen offset:1024 nt\n"
    "s_waitcnt lgkmcnt(9)\n"
    "v_mfma_f32_16x16x32_bf16 v[144:147], a[108:111], v[88:91], v[144:147]\n"
    "ds_read_b64_tr_b16 a[166:167], v246 offset:55296\n"
    "s_mov_b64 vcc, s[44:45]\n"
    "v_mfma_f32_16x16x32_bf16 v[148:151], a[96:99], v[68:71], v[148:151]\n"
    "ds_read_b64_tr_b16 a[168:169], v246 offset:57344\n"
    "v_cndmask_b32_dpp v192, v220, v196, vcc quad_perm:[1,0,3,2] row_mask:0xf bank_mask:0xf\n"
    "v_mfma_f32_16x16x32_bf16 v[148:151], a[100:103], v[76:79], v[148:151]\n"
    "ds_read_b64_tr_b16 a[170:171], v246 offset:59392\n"
    "v_cndmask_b32_dpp v193, v221, v197, vcc quad_perm:[1,0,3,2] row_mask:0xf bank_mask:0xf\n"
    "v_mfma_f32_16x16x32_bf16 v[148:151], a[104:107], v[84:87], v[148:151]\n"
    "ds_read_b64_tr_b16 a[172:173], v246 offset:61440\n"
    "v_permlane16_swap_b32 v236, v238\n"
    "v_mfma_f32_16x16x32_bf16 v[148:151], a[108:111], v[92:95], v[148:151]\n"
    "ds_read_b64_tr_b16 a[174:175], v246 offset:63488\n"
    "v_permlane16_swap_b32 v237, v239\n"
    "v_mfma_f32_16x16x32_bf16 v[152:155], a[112:115], v[64:67], v[152:155]\n"
    "ds_read_b64_tr_b16 a[176:177], v247 offset:49152\n"
    "v_cndmask_b32_dpp v194, v222, v198, vcc quad_perm:[1,0,3,2] row_mask:0xf bank_mask:0xf\n"
    "v_mfma_f32_16x16x32_bf16 v[152:155], a[116:119], v[72:75], v[152:155]\n"
    "ds_read_b64_tr_b16 a[178:179], v247 offset:51200\n"
    "v_cndmask_b32_dpp v195, v223, v199, vcc quad_perm:[1,0,3,2] row_mask:0xf bank_mask:0xf\n"
    "v_mfma_f32_16x16x32_bf16 v[152:155], a[120:123], v[80:83], v[152:155]\n"
    "ds_read_b64_tr_b16 a[180:181], v247 offset:53248\n"
    "s_not_b64 vcc, vcc\n"
    "v_mfma_f32_16x16x32_bf16 v[152:155], a[124:127], v[88:91], v[152:155]\n"
    "ds_read_b64_tr_b16 a[182:183], v247 offset:55296\n"
    "v_cndmask_b32_dpp v220, v196, v220, vcc quad_perm:[1,0,3,2] row_mask:0xf bank_mask:0xf\n"
    "v_mfma_f32_16x16x32_bf16 v[156:159], a[112:115], v[68:71], v[156:159]\n"
    "buffer_store_dwordx4 v[204:207], v232, s[28:31], 0 offen offset:1536 nt\n"
    "v_mfma_f32_16x16x32_bf16 v[156:159], a[116:119], v[76:79], v[156:159]\n"
    "ds_read_b64_tr_b16 a[184:185], v247 offset:57344\n"
    "v_cndmask_b32_dpp v221, v197, v221, vcc quad_perm:[1,0,3,2] row_mask:0xf bank_mask:0xf\n"
    "v_mfma_f32_16x16x32_bf16 v[156:159], a[120:123], v[84:87], v[156:159]\n"
    "ds_read_b64_tr_b16 a[186:187], v247 offset:59392\n"
    "ds_read_b64_tr_b16 a[188:189], v247 offset:61440\n"
    "v_mfma_f32_16x16x32_bf16 v[156:159], a[124:127], v[92:95], v[156:159]\n"
    "ds_read_b64_tr_b16 a[190:191], v247 offset:63488\n"
    "v_cndmask_b32_dpp v222, v198, v222, vcc quad_perm:[1,0,3,2] row_mask:0xf bank_mask:0xf\n"
    "v_mfma_f32_16x16x32_bf16 v[128:131], a[128:131], v[96:99], v[128:131]\n"
    "ds_read_b64_tr_b16 a[64:65], v248 offset:0\n"
    "v_cndmask_b32_dpp v223, v199, v223, vcc quad_perm:[1,0,3,2] row_mask:0xf bank_mask:0xf\n"
    "v_mfma_f32_16x16x32_bf16 v[128:131], a[132:135], v[104:107], v[128:131]\n"
    "ds_read_b64_tr_b16 a[66:67], v248 offset:2048\n"
    "s_mov_b64 vcc, s[44:45]\n"
    "v_mfma_f32_16x16x32_bf16 v[128:131], a[136:139], v[112:115], v[128:131]\n"
    "ds_read_b64_tr_b16 a[68:69], v248 offset:4096\n"
    "v_cndmask_b32_dpp v252, v236, v224, vcc quad_perm:[1,0,3,2] row_mask:0xf bank_mask:0xf\n"
    "v_mfma_f32_16x16x32_bf16 v[128:131], a[140:143], v[120:123], v[128:131]\n"
    "ds_read_b64_tr_b16 a[70:71], v248 offset:6144\n"
    "v_cndmask_b32_dpp v253, v237, v225, vcc quad_perm:[1,0,3,2] row_mask:0xf bank_mask:0xf\n"
    "v_mfma_f32_16x16x32_bf16 v[132:135], a[128:131], v[100:103], v[132:135]\n"
    "ds_read_b64_tr_b16 a[72:73], v248 offset:8192\n"
    "v_cndmask_b32_dpp v254, v238, v226, vcc quad_perm:[1,0,3,2] row_mask:0xf bank_mask:0xf\n"
    "v_mfma_f32_16x16x32_bf16 v[132:135], a[132:135], v[108:111], v[132:135]\n"
    "ds_read_b64_tr_b16 a[74:75], v248 offset:10240\n"
    "v_cndmask_b32_dpp v255, v239, v227, vcc quad_perm:[1,0,3,2] row_mask:0xf bank_mask:0xf\n"
    "v_mfma_f32_16x16x32_bf16 v[132:135], a[136:139], v[116:119], v[132:135]\n"
    "ds_read_b64_tr_b16 a[76:77], v248 offset:12288\n"
    "s_not_b64 vcc, vcc\n"
    "v_mfma_f32_16x16x32_bf16 v[132:135], a[140:143], v[124:127], v[132:135]\n"
    "ds_read_b64_tr_b16 a[78:79], v248 offset:14336\n"
    "v_cndmask_b32_dpp v236, v224, v236, vcc quad_perm:[1,0,3,2] row_mask:0xf bank_mask:0xf\n"
    "v_mfma_f32_16x16x32_bf16 v[136:139], a[144:147], v[96:99], v[136:139]\n"
    "ds_read_b64_tr_b16 a[80:81], v249 offset:0\n"
    "v_cndmask_b32_dpp v237, v225, v237, vcc quad_perm:[1,0,3,2] row_mask:0xf bank_mask:0xf\n"
    "s_waitcnt lgkmcnt(8)\n"
    "v_mfma_f32_16x16x32_bf16 v[136:139], a[148:151], v[104:107], v[136:139]\n"
    "ds_read_b64_tr_b16 a[82:83], v249 offset:2048\n"
    "v_cndmask_b32_dpp v238, v226, v238, vcc quad_perm:[1,0,3,2] row_mask:0xf bank_mask:0xf\n"
    "v_mfma_f32_16x16x32_bf16 v[136:139], a[152:155], v[112:115], v[136:139]\n"
    "ds_read_b64_tr_b16 a[84:85], v249 offset:4096\n"
    "v_cndmask_b32_dpp v239, v227, v239, vcc quad_perm:[1,0,3,2] row_mask:0xf bank_mask:0xf\n"
    "v_mfma_f32_16x16x32_bf16 v[136:139], a[156:159], v[120:123], v[136:139]\n"
    "ds_read_b64_tr_b16 a[86:87], v249 offset:6144\n"
    "s_mov_b64 vcc, s[46:47]\n"
    "v_mfma_f32_16x16x32_bf16 v[140:143], a[144:147], v[100:103], v[140:143]\n"
    "ds_read_b64_tr_b16 a[88:89], v249 offset:8192\n"
    "v_mfma_f32_16x16x32_bf16 v[140:143], a[148:151], v[108:111], v[140:143]\n"
    "ds_read_b64_tr_b16 a[90:91], v249 offset:10240\n"
    "v_cndmask_b32_dpp v196, v252, v192, vcc quad_perm:[2,3,0,1] row_mask:0xf bank_mask:0xf\n"
    "v_mfma_f32_16x16x32_bf16 v[140:143], a[152:155], v[116:119], v[140:143]\n"
    "ds_read_b64_tr_b16 a[92:93], v249 offset:12288\n"
    "v_cndmask_b32_dpp v197, v253, v193, vcc quad_perm:[2,3,0,1] row_mask:0xf bank_mask:0xf\n"
    "v_mfma_f32_16x16x32_bf16 v[140:143], a[156:159], v[124:127], v[140:143]\n"
    "ds_read_b64_tr_b16 a[94:95], v249 offset:14336\n"
    "v_cndmask_b32_dpp v198, v254, v194, vcc quad_perm:[2,3,0,1] row_mask:0xf bank_mask:0xf\n"
    "v_mfma_f32_16x16x32_bf16 v[144:147], a[160:163], v[96:99], v[144:147]\n"
    "ds_read_b64_tr_b16 a[96:97], v250 offset:0\n"
    "v_cndmask_b32_dpp v199, v255, v195, vcc quad_perm:[2,3,0,1] row_mask:0xf bank_mask:0xf\n"
    "v_mfma_f32_16x16x32_bf16 v[144:147], a[164:167], v[104:107], v[144:147]\n"
    "ds_read_b64_tr_b16 a[98:99], v250 offset:2048\n"
    "v_cndmask_b32_dpp v200, v236, v220, vcc quad_perm:[2,3,0,1] row_mask:0xf bank_mask:0xf\n"
    "v_mfma_f32_16x16x32_bf16 v[144:147], a[168:171], v[112:115], v[144:147]\n"
    "ds_read_b64_tr_b16 a[100:101], v250 offset:4096\n"
    "v_cndmask_b32_dpp v201, v237, v221, vcc quad_perm:[2,3,0,1] row_mask:0xf bank_mask:0xf\n"
    "v_mfma_f32_16x16x32_bf16 v[144:147], a[172:175], v[120:123], v[144:147]\n"
    "ds_read_b64_tr_b16 a[102:103], v250 offset:6144\n"
    "v_cndmask_b32_dpp v202, v238, v222, vcc quad_perm:[2,3,0,1] row_mask:0xf bank_mask:0xf\n"
    "v_mfma_f32_16x16x32_bf16 v[148:151], a[160:163], v[100:103], v[148:151]\n"
    "ds_read_b64_tr_b16 a[104:105], v250 offset:8192\n"
    "v_mfma_f32_16x16x32_bf16 v[148:151], a[164:167], v[108:111], v[148:151]\n"
    "ds_read_b64_tr_b16 a[106:107], v250 offset:10240\n"
    "v_cndmask_b32_dpp v203, v239, v223, vcc quad_perm:[2,3,0,1] row_mask:0xf bank_mask:0xf\n"
    "v_mfma_f32_16x16x32_bf16 v[148:151], a[168:171], v[116:119], v[148:151]\n"
    "ds_read_b64_tr_b16 a[108:109], v250 offset:12288\n"
    "s_not_b64 vcc, vcc\n"
    "v_mfma_f32_16x16x32_bf16 v[148:151], a[172:175], v[124:127], v[148:151]\n"
    "ds_read_b64_tr_b16 a[110:111], v250 offset:14336\n"
    "v_cndmask_b32_dpp v224, v192, v252, vcc quad_perm:[2,3,0,1] row_mask:0xf bank_mask:0xf\n"
    "v_mfma_f32_16x16x32_bf16 v[152:155], a[176:179], v[96:99], v[152:155]\n"
    "ds_read_b64_tr_b16 a[112:113], v251 offset:0\n"
    "v_cndmask_b32_dpp v225, v193, v253, vcc quad_perm:[2,3,0,1] row_mask:0xf bank_mask:0xf\n"
    "v_mfma_f32_16x16x32_bf16 v[152:155], a[180:183], v[104:107], v[152:155]\n"
    "ds_read_b64_tr_b16 a[114:115], v251 offset:2048\n"
    "v_cndmask_b32_dpp v226, v194, v254, vcc quad_perm:[2,3,0,1] row_mask:0xf bank_mask:0xf\n"
    "v_mfma_f32_16x16x32_bf16 v[152:155], a[184:187], v[112:115], v[152:155]\n"
    "ds_read_b64_tr_b16 a[116:117], v251 offset:4096\n"
    "v_cndmask_b32_dpp v227, v195, v255, vcc quad_perm:[2,3,0,1] row_mask:0xf bank_mask:0xf\n"
    "v_mfma_f32_16x16x32_bf16 v[152:155], a[188:191], v[120:123], v[152:155]\n"
    "ds_read_b64_tr_b16 a[118:119], v251 offset:6144\n"
    "v_cndmask_b32_dpp v236, v220, v236, vcc quad_perm:[2,3,0,1] row_mask:0xf bank_mask:0xf\n"
    "v_mfma_f32_16x16x32_bf16 v[156:159], a[176:179], v[100:103], v[156:159]\n"
    "ds_read_b64_tr_b16 a[120:121], v251 offset:8192\n"
    "v_cndmask_b32_dpp v237, v221, v237, vcc quad_perm:[2,3,0,1] row_mask:0xf bank_mask:0xf\n"
    "v_mfma_f32_16x16x32_bf16 v[156:159], a[180:183], v[108:111], v[156:159]\n"
    "ds_read_b64_tr_b16 a[122:123], v251 offset:10240\n"
    "v_cndmask_b32_dpp v238, v222, v238, vcc quad_perm:[2,3,0,1] row_mask:0xf bank_mask:0xf\n"
    "v_mfma_f32_16x16x32_bf16 v[156:159], a[184:187], v[116:119], v[156:159]\n"
    "ds_read_b64_tr_b16 a[124:125], v251 offset:12288\n"
    "v_cndmask_b32_dpp v239, v223, v239, vcc quad_perm:[2,3,0,1] row_mask:0xf bank_mask:0xf\n"
    "v_mfma_f32_16x16x32_bf16 v[156:159], a[188:191], v[124:127], v[156:159]\n"
    "ds_read_b64_tr_b16 a[126:127], v251 offset:14336\n"
    "s_waitcnt vmcnt(8) lgkmcnt(8)\n"
    "s_barrier\n"
    "v_mfma_f32_16x16x32_bf16 v[160:163], a[64:67], v[0:3], 0\n"
    "buffer_store_dwordx4 v[196:199], v232, s[28:31], 0 offen offset:128 nt\n"
    "ds_read_b64_tr_b16 a[128:129], v248 offset:16384\n"
    "v_mfma_f32_16x16x32_bf16 v[160:163], a[68:71], v[8:11], v[160:163]\n"
    "ds_read_b64_tr_b16 a[130:131], v248 offset:18432\n"
    "ds_read_b64_tr_b16 a[132:133], v248 offset:20480\n"
    "v_mfma_f32_16x16x32_bf16 v[160:163], a[72:75], v[16:19], v[160:163]\n"
    "ds_read_b64_tr_b16 a[134:135], v248 offset:22528\n"
    "v_mul_f32 v128, v128, v216\n"
    "v_mfma_f32_16x16x32_bf16 v[160:163], a[76:79], v[24:27], v[160:163]\n"
    "ds_read_b64_tr_b16 a[136:137], v248 offset:24576\n"
    "v_mul_f32 v129, v129, v216\n"
    "v_mfma_f32_16x16x32_bf16 v[164:167], a[64:67], v[4:7], 0\n"
    "ds_read_b64_tr_b16 a[138:139], v248 offset:26624\n"
    "v_mul_f32 v130, v130, v216\n"
    "v_mfma_f32_16x16x32_bf16 v[164:167], a[68:71], v[12:15], v[164:167]\n"
    "ds_read_b64_tr_b16 a[140:141], v248 offset:28672\n"
    "v_mul_f32 v131, v131, v216\n"
    "v_mfma_f32_16x16x32_bf16 v[164:167], a[72:75], v[20:23], v[164:167]\n"
    "ds_read_b64_tr_b16 a[142:143], v248 offset:30720\n"
    "v_mul_f32 v132, v132, v233\n"
    "v_mfma_f32_16x16x32_bf16 v[164:167], a[76:79], v[28:31], v[164:167]\n"
    "ds_read_b64_tr_b16 a[144:145], v249 offset:16384\n"
    "v_mul_f32 v133, v133, v233\n"
    "v_mfma_f32_16x16x32_bf16 v[168:171], a[80:83], v[0:3], 0\n"
    "ds_read_b64_tr_b16 a[146:147], v249 offset:18432\n"
    "v_mul_f32 v134, v134, v233\n"
    "v_mfma_f32_16x16x32_bf16 v[168:171], a[84:87], v[8:11], v[168:171]\n"
    "ds_read_b64_tr_b16 a[148:149], v249 offset:20480\n"
    "v_mul_f32 v135, v135, v233\n"
    "v_mfma_f32_16x16x32_bf16 v[168:171], a[88:91], v[16:19], v[168:171]\n"
    "ds_read_b64_tr_b16 a[150:151], v249 offset:22528\n"
    "v_cvt_pk_bf16_f32 v220, v128, v129\n"
    "v_mfma_f32_16x16x32_bf16 v[168:171], a[92:95], v[24:27], v[168:171]\n"
    "ds_read_b64_tr_b16 a[152:153], v249 offset:24576\n"
    "v_cvt_pk_bf16_f32 v221, v130, v131\n"
    "v_mfma_f32_16x16x32_bf16 v[172:175], a[80:83], v[4:7], 0\n"
    "ds_read_b64_tr_b16 a[154:155], v249 offset:26624\n"
    "v_cvt_pk_bf16_f32 v222, v132, v133\n"
    "v_mfma_f32_16x16x32_bf16 v[172:175], a[84:87], v[12:15], v[172:175]\n"
    "ds_read_b64_tr_b16 a[156:157], v249 offset:28672\n"
    "v_cvt_pk_bf16_f32 v223, v134, v135\n"
    "v_mfma_f32_16x16x32_bf16 v[172:175], a[88:91], v[20:23], v[172:175]\n"
    "ds_read_b64_tr_b16 a[158:159], v249 offset:30720\n"
    "v_mul_f32 v136, v136, v216\n"
    "v_mfma_f32_16x16x32_bf16 v[172:175], a[92:95], v[28:31], v[172:175]\n"
    "ds_read_b64_tr_b16 a[160:161], v250 offset:16384\n"
    "v_mul_f32 v137, v137, v216\n"
    "v_permlane16_swap_b32 v220, v222\n"
    "v_mfma_f32_16x16x32_bf16 v[176:179], a[96:99], v[0:3], 0\n"
    "ds_read_b64_tr_b16 a[162:163], v250 offset:18432\n"
    "v_permlane16_swap_b32 v221, v223\n"
    "v_mfma_f32_16x16x32_bf16 v[176:179], a[100:103], v[8:11], v[176:179]\n"
    "ds_read_b64_tr_b16 a[164:165], v250 offset:20480\n"
    "v_mul_f32 v138, v138, v216\n"
    "v_mfma_f32_16x16x32_bf16 v[176:179], a[104:107], v[16:19], v[176:179]\n"
    "ds_read_b64_tr_b16 a[166:167], v250 offset:22528\n"
    "v_mul_f32 v139, v139, v216\n"
    "v_mfma_f32_16x16x32_bf16 v[176:179], a[108:111], v[24:27], v[176:179]\n"
    "ds_read_b64_tr_b16 a[168:169], v250 offset:24576\n"
    "v_mul_f32 v140, v140, v233\n"
    "v_mfma_f32_16x16x32_bf16 v[180:183], a[96:99], v[4:7], 0\n"
    "ds_read_b64_tr_b16 a[170:171], v250 offset:26624\n"
    "v_mul_f32 v141, v141, v233\n"
    "v_mfma_f32_16x16x32_bf16 v[180:183], a[100:103], v[12:15], v[180:183]\n"
    "ds_read_b64_tr_b16 a[172:173], v250 offset:28672\n"
    "v_mul_f32 v142, v142, v233\n"
    "v_mfma_f32_16x16x32_bf16 v[180:183], a[104:107], v[20:23], v[180:183]\n"
    "buffer_store_dwordx4 v[200:203], v232, s[28:31], 0 offen offset:640 nt\n"
    "v_mul_f32 v143, v143, v233\n"
    "v_mfma_f32_16x16x32_bf16 v[180:183], a[108:111], v[28:31], v[180:183]\n"
    "ds_read_b64_tr_b16 a[174:175], v250 offset:30720\n"
    "v_cvt_pk_bf16_f32 v192, v136, v137\n"
    "s_waitcnt lgkmcnt(7)\n"
    "v_mfma_f32_16x16x32_bf16 v[184:187], a[112:115], v[0:3], 0\n"
    "ds_read_b64_tr_b16 a[176:177], v251 offset:16384\n"
    "v_cvt_pk_bf16_f32 v193, v138, v139\n"
    "v_mfma_f32_16x16x32_bf16 v[184:187], a[116:119], v[8:11], v[184:187]\n"
    "ds_read_b64_tr_b16 a[178:179], v251 offset:18432\n"
    "v_cvt_pk_bf16_f32 v194, v140, v141\n"
    "v_mfma_f32_16x16x32_bf16 v[184:187], a[120:123], v[16:19], v[184:187]\n"
    "ds_read_b64_tr_b16 a[180:181], v251 offset:20480\n"
    "v_cvt_pk_bf16_f32 v195, v142, v143\n"
    "v_mfma_f32_16x16x32_bf16 v[184:187], a[124:127], v[24:27], v[184:187]\n"
    "ds_read_b64_tr_b16 a[182:183], v251 offset:22528\n"
    "v_mul_f32 v144, v144, v216\n"
    "v_mfma_f32_16x16x32_bf16 v[188:191], a[112:115], v[4:7], 0\n"
    "ds_read_b64_tr_b16 a[184:185], v251 offset:24576\n"
    "v_mul_f32 v145, v145, v216\n"
    "v_mfma_f32_16x16x32_bf16 v[188:191], a[116:119], v[12:15], v[188:191]\n"
    "ds_read_b64_tr_b16 a[186:187], v251 offset:26624\n"
    "v_permlane16_swap_b32 v192, v194\n"
    "v_mfma_f32_16x16x32_bf16 v[188:191], a[120:123], v[20:23], v[188:191]\n"
    "ds_read_b64_tr_b16 a[188:189], v251 offset:28672\n"
    "v_permlane16_swap_b32 v193, v195\n"
    "v_mfma_f32_16x16x32_bf16 v[188:191], a[124:127], v[28:31], v[188:191]\n"
    "ds_read_b64_tr_b16 a[190:191], v251 offset:30720\n"
    "v_mul_f32 v146, v146, v216\n"
    "v_mfma_f32_16x16x32_bf16 v[160:163], a[128:131], v[32:35], v[160:163]\n"
    "ds_read_b64_tr_b16 a[64:65], v240 offset:0\n"
    "v_mul_f32 v147, v147, v216\n"
    "v_mfma_f32_16x16x32_bf16 v[160:163], a[132:135], v[40:43], v[160:163]\n"
    "ds_read_b64_tr_b16 a[66:67], v240 offset:2048\n"
    "v_mul_f32 v148, v148, v233\n"
    "v_mfma_f32_16x16x32_bf16 v[160:163], a[136:139], v[48:51], v[160:163]\n"
    "ds_read_b64_tr_b16 a[68:69], v240 offset:4096\n"
    "v_mul_f32 v149, v149, v233\n"
    "v_mfma_f32_16x16x32_bf16 v[160:163], a[140:143], v[56:59], v[160:163]\n"
    "ds_read_b64_tr_b16 a[70:71], v240 offset:6144\n"
    "v_mul_f32 v150, v150, v233\n"
    "v_mfma_f32_16x16x32_bf16 v[164:167], a[128:131], v[36:39], v[164:167]\n"
    "ds_read_b64_tr_b16 a[72:73], v240 offset:8192\n"
    "v_mul_f32 v151, v151, v233\n"
    "v_mfma_f32_16x16x32_bf16 v[164:167], a[132:135], v[44:47], v[164:167]\n"
    "ds_read_b64_tr_b16 a[74:75], v240 offset:10240\n"
    "v_cvt_pk_bf16_f32 v252, v144, v145\n"
    "v_mfma_f32_16x16x32_bf16 v[164:167], a[136:139], v[52:55], v[164:167]\n"
    "ds_read_b64_tr_b16 a[76:77], v240 offset:12288\n"
    "v_cvt_pk_bf16_f32 v253, v146, v147\n"
    "v_mfma_f32_16x16x32_bf16 v[164:167], a[140:143], v[60:63], v[164:167]\n"
    "ds_read_b64_tr_b16 a[78:79], v240 offset:14336\n"
    "v_cvt_pk_bf16_f32 v254, v148, v149\n"
    "v_mfma_f32_16x16x32_bf16 v[168:171], a[144:147], v[32:35], v[168:171]\n"
    "ds_read_b64_tr_b16 a[80:81], v241 offset:0\n"
    "v_cvt_pk_bf16_f32 v255, v150, v151\n"
    "v_mfma_f32_16x16x32_bf16 v[168:171], a[148:151], v[40:43], v[168:171]\n"
    "ds_read_b64_tr_b16 a[82:83], v241 offset:2048\n"
    "v_mul_f32 v152, v152, v216\n"
    "v_mul_f32 v153, v153, v216\n"
    "v_mfma_f32_16x16x32_bf16 v[168:171], a[152:155], v[48:51], v[168:171]\n"
    "ds_read_b64_tr_b16 a[84:85], v241 offset:4096\n"
    "v_permlane16_swap_b32 v252, v254\n"
    "v_mfma_f32_16x16x32_bf16 v[168:171], a[156:159], v[56:59], v[168:171]\n"
    "ds_read_b64_tr_b16 a[86:87], v241 offset:6144\n"
    "v_permlane16_swap_b32 v253, v255\n"
    "v_mfma_f32_16x16x32_bf16 v[172:175], a[144:147], v[36:39], v[172:175]\n"
    "buffer_store_dwordx4 v[224:227], v232, s[28:31], 0 offen offset:1152 nt\n"
    "ds_read_b64_tr_b16 a[88:89], v241 offset:8192\n"
    "v_mul_f32 v154, v154, v216\n"
    "v_mfma_f32_16x16x32_bf16 v[172:175], a[148:151], v[44:47], v[172:175]\n"
    "ds_read_b64_tr_b16 a[90:91], v241 offset:10240\n"
    "v_mul_f32 v155, v155, v216\n"
    "v_mfma_f32_16x16x32_bf16 v[172:175], a[152:155], v[52:55], v[172:175]\n"
    "ds_read_b64_tr_b16 a[92:93], v241 offset:12288\n"
    "v_mul_f32 v156, v156, v233\n"
    "v_mfma_f32_16x16x32_bf16 v[172:175], a[156:159], v[60:63], v[172:175]\n"
    "ds_read_b64_tr_b16 a[94:95], v241 offset:14336\n"
    "v_mul_f32 v157, v157, v233\n"
    "s_waitcnt lgkmcnt(7)\n"
    "v_mfma_f32_16x16x32_bf16 v[176:179], a[160:163], v[32:35], v[176:179]\n"
    "ds_read_b64_tr_b16 a[96:97], v242 offset:0\n"
    "v_mul_f32 v158, v158, v233\n"
    "v_mfma_f32_16x16x32_bf16 v[176:179], a[164:167], v[40:43], v[176:179]\n"
    "ds_read_b64_tr_b16 a[98:99], v242 offset:2048\n"
    "v_mul_f32 v159, v159, v233\n"
    "v_mfma_f32_16x16x32_bf16 v[176:179], a[168:171], v[48:51], v[176:179]\n"
    "ds_read_b64_tr_b16 a[100:101], v242 offset:4096\n"
    "v_cvt_pk_bf16_f32 v204, v152, v153\n"
    "v_mfma_f32_16x16x32_bf16 v[176:179], a[172:175], v[56:59], v[176:179]\n"
    "ds_read_b64_tr_b16 a[102:103], v242 offset:6144\n"
    "v_cvt_pk_bf16_f32 v205, v154, v155\n"
    "v_mfma_f32_16x16x32_bf16 v[180:183], a[160:163], v[36:39], v[180:183]\n"
    "ds_read_b64_tr_b16 a[104:105], v242 offset:8192\n"
    "v_cvt_pk_bf16_f32 v206, v156, v157\n"
    "v_mfma_f32_16x16x32_bf16 v[180:183], a[164:167], v[44:47], v[180:183]\n"
    "ds_read_b64_tr_b16 a[106:107], v242 offset:10240\n"
    "v_cvt_pk_bf16_f32 v207, v158, v159\n"
    "v_mfma_f32_16x16x32_bf16 v[180:183], a[168:171], v[52:55], v[180:183]\n"
    "ds_read_b64_tr_b16 a[108:109], v242 offset:12288\n"
    "s_mov_b64 vcc, s[44:45]\n"
    "v_mfma_f32_16x16x32_bf16 v[180:183], a[172:175], v[60:63], v[180:183]\n"
    "ds_read_b64_tr_b16 a[110:111], v242 offset:14336\n"
    "v_cndmask_b32_dpp v196, v192, v220, vcc quad_perm:[1,0,3,2] row_mask:0xf bank_mask:0xf\n"
    "v_mfma_f32_16x16x32_bf16 v[184:187], a[176:179], v[32:35], v[184:187]\n"
    "ds_read_b64_tr_b16 a[112:113], v243 offset:0\n"
    "v_cndmask_b32_dpp v197, v193, v221, vcc quad_perm:[1,0,3,2] row_mask:0xf bank_mask:0xf\n"
    "v_permlane16_swap_b32 v204, v206\n"
    "v_mfma_f32_16x16x32_bf16 v[184:187], a[180:183], v[40:43], v[184:187]\n"
    "ds_read_b64_tr_b16 a[114:115], v243 offset:2048\n"
    "v_permlane16_swap_b32 v205, v207\n"
    "v_mfma_f32_16x16x32_bf16 v[184:187], a[184:187], v[48:51], v[184:187]\n"
    "ds_read_b64_tr_b16 a[116:117], v243 offset:4096\n"
    "v_cndmask_b32_dpp v198, v194, v222, vcc quad_perm:[1,0,3,2] row_mask:0xf bank_mask:0xf\n"
    "v_mfma_f32_16x16x32_bf16 v[184:187], a[188:191], v[56:59], v[184:187]\n"
    "ds_read_b64_tr_b16 a[118:119], v243 offset:6144\n"
    "v_cndmask_b32_dpp v199, v195, v223, vcc quad_perm:[1,0,3,2] row_mask:0xf bank_mask:0xf\n"
    "v_mfma_f32_16x16x32_bf16 v[188:191], a[176:179], v[36:39], v[188:191]\n"
    "ds_read_b64_tr_b16 a[120:121], v243 offset:8192\n"
    "s_not_b64 vcc, vcc\n"
    "v_mfma_f32_16x16x32_bf16 v[188:191], a[180:183], v[44:47], v[188:191]\n"
    "ds_read_b64_tr_b16 a[122:123], v243 offset:10240\n"
    "v_cndmask_b32_dpp v192, v220, v192, vcc quad_perm:[1,0,3,2] row_mask:0xf bank_mask:0xf\n"
    "v_mfma_f32_16x16x32_bf16 v[188:191], a[184:187], v[52:55], v[188:191]\n"
    "ds_read_b64_tr_b16 a[124:125], v243 offset:12288\n"
    "v_cndmask_b32_dpp v193, v221, v193, vcc quad_perm:[1,0,3,2] row_mask:0xf bank_mask:0xf\n"
    "v_cndmask_b32_dpp v194, v222, v194, vcc quad_perm:[1,0,3,2] row_mask:0xf bank_mask:0xf\n"
    "v_mfma_f32_16x16x32_bf16 v[188:191], a[188:191], v[60:63], v[188:191]\n"
    "ds_read_b64_tr_b16 a[126:127], v243 offset:14336\n"
    "v_cndmask_b32_dpp v195, v223, v195, vcc quad_perm:[1,0,3,2] row_mask:0xf bank_mask:0xf\n"
    "s_waitcnt vmcnt(7) lgkmcnt(7)\n"
    "s_barrier\n"
    "v_mfma_f32_16x16x32_bf16 v[160:163], a[64:67], v[64:67], v[160:163]\n"
    "ds_read_b64_tr_b16 a[128:129], v240 offset:16384\n"
    "s_mov_b64 vcc, s[44:45]\n"
    "v_mfma_f32_16x16x32_bf16 v[160:163], a[68:71], v[72:75], v[160:163]\n"
    "ds_read_b64_tr_b16 a[130:131], v240 offset:18432\n"
    "v_cndmask_b32_dpp v200, v204, v252, vcc quad_perm:[1,0,3,2] row_mask:0xf bank_mask:0xf\n"
    "v_mfma_f32_16x16x32_bf16 v[160:163], a[72:75], v[80:83], v[160:163]\n"
    "buffer_store_dwordx4 v[236:239], v232, s[28:31], 0 offen offset:1664 nt\n"
    "v_cndmask_b32_dpp v201, v205, v253, vcc quad_perm:[1,0,3,2] row_mask:0xf bank_mask:0xf\n"
    "v_mfma_f32_16x16x32_bf16 v[160:163], a[76:79], v[88:91], v[160:163]\n"
    "ds_read_b64_tr_b16 a[132:133], v240 offset:20480\n"
    "v_cndmask_b32_dpp v202, v206, v254, vcc quad_perm:[1,0,3,2] row_mask:0xf bank_mask:0xf\n"
    "v_mfma_f32_16x16x32_bf16 v[164:167], a[64:67], v[68:71], v[164:167]\n"
    "ds_read_b64_tr_b16 a[134:135], v240 offset:22528\n"
    "v_cndmask_b32_dpp v203, v207, v255, vcc quad_perm:[1,0,3,2] row_mask:0xf bank_mask:0xf\n"
    "s_not_b64 vcc, vcc\n"
    "v_mfma_f32_16x16x32_bf16 v[164:167], a[68:71], v[76:79], v[164:167]\n"
    "ds_read_b64_tr_b16 a[136:137], v240 offset:24576\n"
    "v_cndmask_b32_dpp v204, v252, v204, vcc quad_perm:[1,0,3,2] row_mask:0xf bank_mask:0xf\n"
    "v_mfma_f32_16x16x32_bf16 v[164:167], a[72:75], v[84:87], v[164:167]\n"
    "ds_read_b64_tr_b16 a[138:139], v240 offset:26624\n"
    "v_cndmask_b32_dpp v205, v253, v205, vcc quad_perm:[1,0,3,2] row_mask:0xf bank_mask:0xf\n"
    "v_mfma_f32_16x16x32_bf16 v[164:167], a[76:79], v[92:95], v[164:167]\n"
    "ds_read_b64_tr_b16 a[140:141], v240 offset:28672\n"
    "v_cndmask_b32_dpp v206, v254, v206, vcc quad_perm:[1,0,3,2] row_mask:0xf bank_mask:0xf\n"
    "v_mfma_f32_16x16x32_bf16 v[168:171], a[80:83], v[64:67], v[168:171]\n"
    "ds_read_b64_tr_b16 a[142:143], v240 offset:30720\n"
    "v_cndmask_b32_dpp v207, v255, v207, vcc quad_perm:[1,0,3,2] row_mask:0xf bank_mask:0xf\n"
    "v_mfma_f32_16x16x32_bf16 v[168:171], a[84:87], v[72:75], v[168:171]\n"
    "ds_read_b64_tr_b16 a[144:145], v241 offset:16384\n"
    "s_mov_b64 vcc, s[46:47]\n"
    "v_cndmask_b32_dpp v220, v200, v196, vcc quad_perm:[2,3,0,1] row_mask:0xf bank_mask:0xf\n"
    "v_mfma_f32_16x16x32_bf16 v[168:171], a[88:91], v[80:83], v[168:171]\n"
    "ds_read_b64_tr_b16 a[146:147], v241 offset:18432\n"
    "v_cndmask_b32_dpp v221, v201, v197, vcc quad_perm:[2,3,0,1] row_mask:0xf bank_mask:0xf\n"
    "v_mfma_f32_16x16x32_bf16 v[168:171], a[92:95], v[88:91], v[168:171]\n"
    "ds_read_b64_tr_b16 a[148:149], v241 offset:20480\n"
    "v_cndmask_b32_dpp v222, v202, v198, vcc quad_perm:[2,3,0,1] row_mask:0xf bank_mask:0xf\n"
    "v_mfma_f32_16x16x32_bf16 v[172:175], a[80:83], v[68:71], v[172:175]\n"
    "ds_read_b64_tr_b16 a[150:151], v241 offset:22528\n"
    "v_cndmask_b32_dpp v223, v203, v199, vcc quad_perm:[2,3,0,1] row_mask:0xf bank_mask:0xf\n"
    "v_mfma_f32_16x16x32_bf16 v[172:175], a[84:87], v[76:79], v[172:175]\n"
    "ds_read_b64_tr_b16 a[152:153], v241 offset:24576\n"
    "v_cndmask_b32_dpp v224, v204, v192, vcc quad_perm:[2,3,0,1] row_mask:0xf bank_mask:0xf\n"
    "v_mfma_f32_16x16x32_bf16 v[172:175], a[88:91], v[84:87], v[172:175]\n"
    "ds_read_b64_tr_b16 a[154:155], v241 offset:26624\n"
    "PAD5_%=:\ns_cbranch_execz PADRET_%=\n"
    "v_cndmask_b32_dpp v225, v205, v193, vcc quad_perm:[2,3,0,1] row_mask:0xf bank_mask:0xf\n"
    "v_mfma_f32_16x16x32_bf16 v[172:175], a[92:95], v[92:95], v[172:175]\n"
    "ds_read_b64_tr_b16 a[156:157], v241 offset:28672\n"
    "v_cndmask_b32_dpp v226, v206, v194, vcc quad_perm:[2,3,0,1] row_mask:0xf bank_mask:0xf\n"
    "v_cndmask_b32_dpp v227, v207, v195, vcc quad_perm:[2,3,0,1] row_mask:0xf bank_mask:0xf\n"
    "v_mfma_f32_16x16x32_bf16 v[176:179], a[96:99], v[64:67], v[176:179]\n"
    "ds_read_b64_tr_b16 a[158:159], v241 offset:30720\n"
    "s_not_b64 vcc, vcc\n"
    "v_mfma_f32_16x16x32_bf16 v[176:179], a[100:103], v[72:75], v[176:179]\n"
    "ds_read_b64_tr_b16 a[160:161], v242 offset:16384\n"
    "v_cndmask_b32_dpp v252, v196, v200, vcc quad_perm:[2,3,0,1] row_mask:0xf bank_mask:0xf\n"
    "v_mfma_f32_16x16x32_bf16 v[176:179], a[104:107], v[80:83], v[176:179]\n"
    "ds_read_b64_tr_b16 a[162:163], v242 offset:18432\n"
    "v_cndmask_b32_dpp v253, v197, v201, vcc quad_perm:[2,3,0,1] row_mask:0xf bank_mask:0xf\n"
    "v_mfma_f32_16x16x32_bf16 v[176:179], a[108:111], v[88:91], v[176:179]\n"
    "ds_read_b64_tr_b16 a[164:165], v242 offset:20480\n"
    "v_cndmask_b32_dpp v254, v198, v202, vcc quad_perm:[2,3,0,1] row_mask:0xf bank_mask:0xf\n"
    "v_mfma_f32_16x16x32_bf16 v[180:183], a[96:99], v[68:71], v[180:183]\n"
    "ds_read_b64_tr_b16 a[166:167], v242 offset:22528\n"
    "v_cndmask_b32_dpp v255, v199, v203, vcc quad_perm:[2,3,0,1] row_mask:0xf bank_mask:0xf\n"
    "v_mfma_f32_16x16x32_bf16 v[180:183], a[100:103], v[76:79], v[180:183]\n"
    "ds_read_b64_tr_b16 a[168:169], v242 offset:24576\n"
    "v_cndmask_b32_dpp v204, v192, v204, vcc quad_perm:[2,3,0,1] row_mask:0xf bank_mask:0xf\n"
    "v_cndmask_b32_dpp v205, v193, v205, vcc quad_perm:[2,3,0,1] row_mask:0xf bank_mask:0xf\n"
    "v_mfma_f32_16x16x32_bf16 v[180:183], a[104:107], v[84:87], v[180:183]\n"
    "ds_read_b64_tr_b16 a[170:171], v242 offset:26624\n"
    "v_cndmask_b32_dpp v206, v194, v206, vcc quad_perm:[2,3,0,1] row_mask:0xf bank_mask:0xf\n"
    "v_mfma_f32_16x16x32_bf16 v[180:183], a[108:111], v[92:95], v[180:183]\n"
    "ds_read_b64_tr_b16 a[172:173], v242 offset:28672\n"
    "v_cndmask_b32_dpp v207, v195, v207, vcc quad_perm:[2,3,0,1] row_mask:0xf bank_mask:0xf\n"
    "s_waitcnt lgkmcnt(8)\n"
    "v_mfma_f32_16x16x32_bf16 v[184:187], a[112:115], v[64:67], v[184:187]\n"
    "buffer_store_dwordx4 v[220:223], v232, s[28:31], 0 offen offset:256 nt\n"
    "v_mfma_f32_16x16x32_bf16 v[184:187], a[116:119], v[72:75], v[184:187]\n"
    "ds_read_b64_tr_b16 a[174:175], v242 offset:30720\n"
    "ds_read_b64_tr_b16 a[176:177], v243 offset:16384\n"
    "v_mfma_f32_16x16x32_bf16 v[184:187], a[120:123], v[80:83], v[184:187]\n"
    "ds_read_b64_tr_b16 a[178:179], v243 offset:18432\n"
    "ds_read_b64_tr_b16 a[180:181], v243 offset:20480\n"
    "v_mfma_f32_16x16x32_bf16 v[184:187], a[124:127], v[88:91], v[184:187]\n"
    "ds_read_b64_tr_b16 a[182:183], v243 offset:22528\n"
    "v_mfma_f32_16x16x32_bf16 v[188:191], a[112:115], v[68:71], v[188:191]\n"
    "ds_read_b64_tr_b16 a[184:185], v243 offset:24576\n"
    "v_mfma_f32_16x16x32_bf16 v[188:191], a[116:119], v[76:79], v[188:191]\n"
    "ds_read_b64_tr_b16 a[186:187], v243 offset:26624\n"
    "v_mfma_f32_16x16x32_bf16 v[188:191], a[120:123], v[84:87], v[188:191]\n"
    "ds_read_b64_tr_b16 a[188:189], v243 offset:28672\n"
    "v_mfma_f32_16x16x32_bf16 v[188:191], a[124:127], v[92:95], v[188:191]\n"
    "ds_read_b64_tr_b16 a[190:191], v243 offset:30720\n"
    "v_mfma_f32_16x16x32_bf16 v[160:163], a[128:131], v[96:99], v[160:163]\n"
    "buffer_store_dwordx4 v[224:227], v232, s[28:31], 0 offen offset:768 nt\n"
    "v_mfma_f32_16x16x32_bf16 v[160:163], a[132:135], v[104:107], v[160:163]\n"
    "v_mfma_f32_16x16x32_bf16 v[160:163], a[136:139], v[112:115], v[160:163]\n"
    "v_mfma_f32_16x16x32_bf16 v[160:163], a[140:143], v[120:123], v[160:163]\n"
    "v_mfma_f32_16x16x32_bf16 v[164:167], a[128:131], v[100:103], v[164:167]\n"
    "v_mfma_f32_16x16x32_bf16 v[164:167], a[132:135], v[108:111], v[164:167]\n"
    "v_mfma_f32_16x16x32_bf16 v[164:167], a[136:139], v[116:119], v[164:167]\n"
    "buffer_store_dwordx4 v[252:255], v232, s[28:31], 0 offen offset:1280 nt\n"
    "v_mfma_f32_16x16x32_bf16 v[164:167], a[140:143], v[124:127], v[164:167]\n"
    "v_mfma_f32_16x16x32_bf16 v[168:171], a[144:147], v[96:99], v[168:171]\n"
    "v_mfma_f32_16x16x32_bf16 v[168:171], a[148:151], v[104:107], v[168:171]\n"
    "v_mfma_f32_16x16x32_bf16 v[168:171], a[152:155], v[112:115], v[168:171]\n"
    "v_mul_f32 v160, v160, v216\n"
    "v_mul_f32 v161, v161, v216\n"
    "s_waitcnt lgkmcnt(2)\n"
    "v_mfma_f32_16x16x32_bf16 v[168:171], a[156:159], v[120:123], v[168:171]\n"
    "v_mul_f32 v162, v162, v216\n"
    "v_mul_f32 v163, v163, v216\n"
    "v_mfma_f32_16x16x32_bf16 v[172:175], a[144:147], v[100:103], v[172:175]\n"
    "buffer_store_dwordx4 v[204:207], v232, s[28:31], 0 offen offset:1792 nt\n"
    "v_mul_f32 v164, v164, v233\n"
    "v_mfma_f32_16x16x32_bf16 v[172:175], a[148:151], v[108:111], v[172:175]\n"
    "v_mul_f32 v165, v165, v233\n"
    "v_mul_f32 v166, v166, v233\n"
    "v_mfma_f32_16x16x32_bf16 v[172:175], a[152:155], v[116:119], v[172:175]\n"
    "v_mul_f32 v167, v167, v233\n"
    "v_cvt_pk_bf16_f32 v192, v160, v161\n"
    "v_mfma_f32_16x16x32_bf16 v[172:175], a[156:159], v[124:127], v[172:175]\n"
    "v_cvt_pk_bf16_f32 v193, v162, v163\n"
    "v_cvt_pk_bf16_f32 v194, v164, v165\n"
    "v_mfma_f32_16x16x32_bf16 v[176:179], a[160:163], v[96:99], v[176:179]\n"
    "v_cvt_pk_bf16_f32 v195, v166, v167\n"
    "v_permlane16_swap_b32 v192, v194\n"
    "v_mfma_f32_16x16x32_bf16 v[176:179], a[164:167], v[104:107], v[176:179]\n"
    "v_permlane16_swap_b32 v193, v195\n"
    "v_mfma_f32_16x16x32_bf16 v[176:179], a[168:171], v[112:115], v[176:179]\n"
    "v_mul_f32 v168, v168, v216\n"
    "v_mul_f32 v169, v169, v216\n"
    "v_mfma_f32_16x16x32_bf16 v[176:179], a[172:175], v[120:123], v[176:179]\n"
    "v_mul_f32 v170, v170, v216\n"
    "v_mul_f32 v171, v171, v216\n"
    "v_mul_f32 v172, v172, v233\n"
    "v_mfma_f32_16x16x32_bf16 v[180:183], a[160:163], v[100:103], v[180:183]\n"
    "v_mul_f32 v173, v173, v233\n"
    "v_mul_f32 v174, v174, v233\n"
    "v_mfma_f32_16x16x32_bf16 v[180:183], a[164:167], v[108:111], v[180:183]\n"
    "v_mul_f32 v175, v175, v233\n"
    "v_cvt_pk_bf16_f32 v196, v168, v169\n"
    "v_mfma_f32_16x16x32_bf16 v[180:183], a[168:171], v[116:119], v[180:183]\n"
    "v_cvt_pk_bf16_f32 v197, v170, v171\n"
    "v_cvt_pk_bf16_f32 v198, v172, v173\n"
    "v_cvt_pk_bf16_f32 v199, v174, v175\n"
    "v_mfma_f32_16x16x32_bf16 v[180:183], a[172:175], v[124:127], v[180:183]\n"
    "v_permlane16_swap_b32 v196, v198\n"
    "v_permlane16_swap_b32 v197, v199\n"
    "v_mfma_f32_16x16x32_bf16 v[184:187], a[176:179], v[96:99], v[184:187]\n"
    "s_mov_b64 vcc, s[44:45]\n"
    "v_cndmask_b32_dpp v220, v196, v192, vcc quad_perm:[1,0,3,2] row_mask:0xf bank_mask:0xf\n"
    "v_cndmask_b32_dpp v221, v197, v193, vcc quad_perm:[1,0,3,2] row_mask:0xf bank_mask:0xf\n"
    "v_mfma_f32_16x16x32_bf16 v[184:187], a[180:183], v[104:107], v[184:187]\n"
    "v_cndmask_b32_dpp v222, v198, v194, vcc quad_perm:[1,0,3,2] row_mask:0xf bank_mask:0xf\n"
    "v_cndmask_b32_dpp v223, v199, v195, vcc quad_perm:[1,0,3,2] row_mask:0xf bank_mask:0xf\n"
    "v_mfma_f32_16x16x32_bf16 v[184:187], a[184:187], v[112:115], v[184:187]\n"
    "s_not_b64 vcc, vcc\n"
    "v_cndmask_b32_dpp v196, v192, v196, vcc quad_perm:[1,0,3,2] row_mask:0xf bank_mask:0xf\n"
    "v_cndmask_b32_dpp v197, v193, v197, vcc quad_perm:[1,0,3,2] row_mask:0xf bank_mask:0xf\n"
    "v_cndmask_b32_dpp v198, v194, v198, vcc quad_perm:[1,0,3,2] row_mask:0xf bank_mask:0xf\n"
    "s_waitcnt lgkmcnt(0)\n"
    "v_mfma_f32_16x16x32_bf16 v[184:187], a[188:191], v[120:123], v[184:187]\n"
    "v_cndmask_b32_dpp v199, v195, v199, vcc quad_perm:[1,0,3,2] row_mask:0xf bank_mask:0xf\n"
    "v_mul_f32 v176, v176, v216\n"
    "v_mul_f32 v177, v177, v216\n"
    "v_mul_f32 v178, v178, v216\n"
    "v_mfma_f32_16x16x32_bf16 v[188:191], a[176:179], v[100:103], v[188:191]\n"
    "v_mul_f32 v179, v179, v216\n"
    "v_mul_f32 v180, v180, v233\n"
    "v_mul_f32 v181, v181, v233\n"
    "v_mfma_f32_16x16x32_bf16 v[188:191], a[180:183], v[108:111], v[188:191]\n"
    "v_mul_f32 v182, v182, v233\n"
    "v_mul_f32 v183, v183, v233\n"
    "v_cvt_pk_bf16_f32 v200, v176, v177\n"
    "v_cvt_pk_bf16_f32 v201, v178, v179\n"
    "v_mfma_f32_16x16x32_bf16 v[188:191], a[184:187], v[116:119], v[188:191]\n"
    "v_cvt_pk_bf16_f32 v202, v180, v181\n"
    "v_cvt_pk_bf16_f32 v203, v182, v183\n"
    "s_nop 0\n"
    "v_permlane16_swap_b32 v200, v202\n"
    "v_permlane16_swap_b32 v201, v203\n"
    "v_mfma_f32_16x16x32_bf16 v[188:191], a[188:191], v[124:127], v[188:191]\n"
    "v_mul_f32 v184, v184, v216\n"
    "v_mul_f32 v185, v185, v216\n"
    "v_mul_f32 v186, v186, v216\n"
    "v_mul_f32 v187, v187, v216\n"
    "s_nop 3\n"
    "v_mul_f32 v188, v188, v233\n"
    "v_mul_f32 v189, v189, v233\n"
    "v_mul_f32 v190, v190, v233\n"
    "v_mul_f32 v191, v191, v233\n"
    "v_cvt_pk_bf16_f32 v236, v184, v185\n"
    "v_cvt_pk_bf16_f32 v237, v186, v187\n"
    "v_cvt_pk_bf16_f32 v238, v188, v189\n"
    "v_cvt_pk_bf16_f32 v239, v190, v191\n"
    "s_nop 0\n"
    "v_permlane16_swap_b32 v236, v238\n"
    "v_permlane16_swap_b32 v237, v239\n"
    "s_mov_b64 vcc, s[44:45]\n"
    "v_cndmask_b32_dpp v224, v236, v200, vcc quad_perm:[1,0,3,2] row_mask:0xf bank_mask:0xf\n"
    "v_cndmask_b32_dpp v225, v237, v201, vcc quad_perm:[1,0,3,2] row_mask:0xf bank_mask:0xf\n"
    "v_cndmask_b32_dpp v226, v238, v202, vcc quad_perm:[1,0,3,2] row_mask:0xf bank_mask:0xf\n"
    "v_cndmask_b32_dpp v227, v239, v203, vcc quad_perm:[1,0,3,2] row_mask:0xf bank_mask:0xf\n"
    "s_not_b64 vcc, vcc\n"
    "v_cndmask_b32_dpp v236, v200, v236, vcc quad_perm:[1,0,3,2] row_mask:0xf bank_mask:0xf\n"
    "v_cndmask_b32_dpp v237, v201, v237, vcc quad_perm:[1,0,3,2] row_mask:0xf bank_mask:0xf\n"
    "v_cndmask_b32_dpp v238, v202, v238, vcc quad_perm:[1,0,3,2] row_mask:0xf bank_mask:0xf\n"
    "v_cndmask_b32_dpp v239, v203, v239, vcc quad_perm:[1,0,3,2] row_mask:0xf bank_mask:0xf\n"
    "s_mov_b64 vcc, s[46:47]\n"
    "v_cndmask_b32_dpp v192, v224, v220, vcc quad_perm:[2,3,0,1] row_mask:0xf bank_mask:0xf\n"
    "v_cndmask_b32_dpp v193, v225, v221, vcc quad_perm:[2,3,0,1] row_mask:0xf bank_mask:0xf\n"
    "v_cndmask_b32_dpp v194, v226, v222, vcc quad_perm:[2,3,0,1] row_mask:0xf bank_mask:0xf\n"
    "v_cndmask_b32_dpp v195, v227, v223, vcc quad_perm:[2,3,0,1] row_mask:0xf bank_mask:0xf\n"
    "buffer_store_dwordx4 v[192:195], v232, s[28:31], 0 offen offset:384 nt\n"
    "v_cndmask_b32_dpp v252, v236, v196, vcc quad_perm:[2,3,0,1] row_mask:0xf bank_mask:0xf\n"
    "v_cndmask_b32_dpp v253, v237, v197, vcc quad_perm:[2,3,0,1] row_mask:0xf bank_mask:0xf\n"
    "v_cndmask_b32_dpp v254, v238, v198, vcc quad_perm:[2,3,0,1] row_mask:0xf bank_mask:0xf\n"
    "v_cndmask_b32_dpp v255, v239, v199, vcc quad_perm:[2,3,0,1] row_mask:0xf bank_mask:0xf\n"
    "buffer_store_dwordx4 v[252:255], v232, s[28:31], 0 offen offset:896 nt\n"
    "s_not_b64 vcc, vcc\n"
    "v_cndmask_b32_dpp v200, v220, v224, vcc quad_perm:[2,3,0,1] row_mask:0xf bank_mask:0xf\n"
    "v_cndmask_b32_dpp v201, v221, v225, vcc quad_perm:[2,3,0,1] row_mask:0xf bank_mask:0xf\n"
    "v_cndmask_b32_dpp v202, v222, v226, vcc quad_perm:[2,3,0,1] row_mask:0xf bank_mask:0xf\n"
    "v_cndmask_b32_dpp v203, v223, v227, vcc quad_perm:[2,3,0,1] row_mask:0xf bank_mask:0xf\n"
    "buffer_store_dwordx4 v[200:203], v232, s[28:31], 0 offen offset:1408 nt\n"
    "v_cndmask_b32_dpp v236, v196, v236, vcc quad_perm:[2,3,0,1] row_mask:0xf bank_mask:0xf\n"
    "v_cndmask_b32_dpp v237, v197, v237, vcc quad_perm:[2,3,0,1] row_mask:0xf bank_mask:0xf\n"
    "v_cndmask_b32_dpp v238, v198, v238, vcc quad_perm:[2,3,0,1] row_mask:0xf bank_mask:0xf\n"
    "v_cndmask_b32_dpp v239, v199, v239, vcc quad_perm:[2,3,0,1] row_mask:0xf bank_mask:0xf\n"
    "buffer_store_dwordx4 v[236:239], v232, s[28:31], 0 offen offset:1920 nt\n"
    "s_cmp_lg_u64 s[48:49], 0\n"
    "s_cbranch_scc1 SLOW_%=\n"
    "s_endpgm\n"
    "CODEEND_%=:\n"
    ".p2align 12\n"
    "SLOW_%=:\n"
    "s_waitcnt vmcnt(0) lgkmcnt(0)\n"
    : "+s"(ka), "+s"(bid), "+s"(wave)
    : "s"((unsigned)(size_t)lds)
    : "v0", "v1", "v2", "v3", "v4", "v5", "v6", "v7", "v8", "v9", "v10", "v11", "v12", "v13", "v14", "v15", "v16", "v17", "v18", "v19", "v20", "v21", "v22", "v23", "v24", "v25", "v26", "v27", "v28", "v29", "v30", "v31", "v32", "v33", "v34", "v35", "v36", "v37", "v38", "v39", "v40", "v41", "v42", "v43", "v44", "v45", "v46", "v47", "v48", "v49", "v50", "v51", "v52", "v53", "v54", "v55", "v56", "v57", "v58", "v59", "v60", "v61", "v62", "v63", "v64", "v65", "v66", "v67", "v68", "v69", "v70", "v71", "v72", "v73", "v74", "v75", "v76", "v77", "v78", "v79", "v80", "v81", "v82", "v83", "v84", "v85", "v86", "v87", "v88", "v89", "v90", "v91", "v92", "v93", "v94", "v95", "v96", "v97", "v98", "v99", "v100", "v101", "v102", "v103", "v104", "v105", "v106", "v107", "v108", "v109", "v110", "v111", "v112", "v113", "v114", "v115", "v116", "v117", "v118", "v119", "v120", "v121", "v122", "v123", "v124", "v125", "v126", "v127", "v128", "v129", "v130", "v131", "v132", "v133", "v134", "v135", "v136", "v137", "v138", "v139", "v140", "v141", "v142", "v143", "v144", "v145", "v146", "v147", "v148", "v149", "v150", "v151", "v152", "v153", "v154", "v155", "v156", "v157", "v158", "v159", "v160", "v161", "v162", "v163", "v164", "v165", "v166", "v167", "v168", "v169", "v170", "v171", "v172", "v173", "v174", "v175", "v176", "v177", "v178", "v179", "v180", "v181", "v182", "v183", "v184", "v185", "v186", "v187", "v188", "v189", "v190", "v191", "v192", "v193", "v194", "v195", "v196", "v197", "v198", "v199", "v200", "v201", "v202", "v203", "v204", "v205", "v206", "v207", "v208", "v209", "v210", "v211", "v212", "v213", "v214", "v215", "v216", "v217", "v218", "v219", "v220", "v221", "v222", "v223", "v224", "v225", "v226", "v227", "v228", "v229", "v230", "v231", "v232", "v233", "v234", "v235", "v236", "v237", "v238", "v239", "v240", "v241", "v242", "v243", "v244", "v245", "v246", "v247", "v248", "v249", "v250", "v251", "v252", "v253", "v254", "v255", "a0", "a1", "a2", "a3", "a4", "a5", "a6", "a7", "a8", "a9", "a10", "a11", "a12", "a13", "a14", "a15", "a16", "a17", "a18", "a19", "a20", "a21", "a22", "a23", "a24", "a25", "a26", "a27", "a28", "a29", "a30", "a31", "a32", "a33", "a34", "a35", "a36", "a37", "a38", "a39", "a40", "a41", "a42", "a43", "a44", "a45", "a46", "a47", "a48", "a49", "a50", "a51", "a52", "a53", "a54", "a55", "a56", "a57", "a58", "a59", "a60", "a61", "a62", "a63", "a64", "a65", "a66", "a67", "a68", "a69", "a70", "a71", "a72", "a73", "a74", "a75", "a76", "a77", "a78", "a79", "a80", "a81", "a82", "a83", "a84", "a85", "a86", "a87", "a88", "a89", "a90", "a91", "a92", "a93", "a94", "a95", "a96", "a97", "a98", "a99", "a100", "a101", "a102", "a103", "a104", "a105", "a106", "a107", "a108", "a109", "a110", "a111", "a112", "a113", "a114", "a115", "a116", "a117", "a118", "a119", "a120", "a121", "a122", "a123", "a124", "a125", "a126", "a127", "a128", "a129", "a130", "a131", "a132", "a133", "a134", "a135", "a136", "a137", "a138", "a139", "a140", "a141", "a142", "a143", "a144", "a145", "a146", "a147", "a148", "a149", "a150", "a151", "a152", "a153", "a154", "a155", "a156", "a157", "a158", "a159", "a160", "a161", "a162", "a163", "a164", "a165", "a166", "a167", "a168", "a169", "a170", "a171", "a172", "a173", "a174", "a175", "a176", "a177", "a178", "a179", "a180", "a181", "a182", "a183", "a184", "a185", "a186", "a187", "a188", "a189", "a190", "a191", "a192", "a193", "a194", "a195", "a196", "a197", "a198", "a199", "a200", "a201", "a202", "a203", "a204", "a205", "a206", "a207", "a208", "a209", "a210", "a211", "a212", "a213", "a214", "a215", "a216", "a217", "a218", "a219", "a220", "a221", "a222", "a223", "a224", "a225", "a226", "a227", "a228", "a229", "a230", "a231", "a232", "a233", "a234", "a235", "a236", "a237", "a238", "a239", "a240", "a241", "a242", "a243", "a244", "a245", "a246", "a247", "a248", "a249", "a250", "a251", "a252", "a253", "a254", "a255", "s8", "s9", "s10", "s11", "s12", "s13", "s14", "s15", "s16", "s17", "s18", "s19", "s20", "s21", "s22", "s23", "s24", "s25", "s26", "s27", "s28", "s29", "s30", "s31", "s32", "s33", "s34", "s35", "s36", "s37", "s38", "s39", "s40", "s41", "s42", "s43", "s44", "s45", "s46", "s47", "s48", "s49", "s50", "s51", "s52", "s53", "s54", "s55", "s56", "s57", "s58", "s59", "s60", "s61", "s62", "s63", "s64", "s65", "s66", "s67", "s68", "s69", "s70", "s71", "s72", "s73", "s74", "s75", "s76", "s77", "s78", "s79", "s80", "s81", "s82", "s83", "s84", "s85", "s86", "s87", "s88", "s89", "s90", "s91", "s92", "s93", "s94", "s95", "s96", "s97", "s98", "s99", "vcc", "scc", "memory");
  const char* const __attribute__((address_space(4)))* arg = (const char* const __attribute__((address_space(4)))*)ka;
  const unsigned lane = __builtin_amdgcn_mbcnt_hi(~0u, __builtin_amdgcn_mbcnt_lo(~0u, 0u));
  const unsigned hh = (bid & 7u) * 32u + (bid >> 3);
  const size_t qo = (size_t)hh * 65536u + wave * 16384u + lane * 8u, ko = (size_t)(hh >> 1) * 262144u + lane * 8u;
  for (int r = 0; r < 32; ++r) {
    const uint2 qq = *(const uint2*)(arg[0] + qo + r * 512);
    const float q0 = __uint_as_float(qq.x << 16), q1 = __uint_as_float(qq.x & 0xffff0000u), q2 = __uint_as_float(qq.y << 16), q3 = __uint_as_float(qq.y & 0xffff0000u);
    float m = -INFINITY, l = 0.f, a0 = 0.f, a1 = 0.f, a2 = 0.f, a3 = 0.f;
    for (int j = 0; j < 512; ++j) {
      const uint2 kk = *(const uint2*)(arg[1] + ko + j * 512), vv = *(const uint2*)(arg[2] + ko + j * 512);
      float s = q0 * __uint_as_float(kk.x << 16) + q1 * __uint_as_float(kk.x & 0xffff0000u) + q2 * __uint_as_float(kk.y << 16) + q3 * __uint_as_float(kk.y & 0xffff0000u);
      for (int x = 32; x; x >>= 1) s += __shfl_xor(s, x);
      s *= 0.0625f;
      const float mn = fmaxf(m, s), f = __expf(m - mn), p = __expf(s - mn);
      l = l * f + p; m = mn;
      a0 = a0 * f + p * __uint_as_float(vv.x << 16); a1 = a1 * f + p * __uint_as_float(vv.x & 0xffff0000u);
      a2 = a2 * f + p * __uint_as_float(vv.y << 16); a3 = a3 * f + p * __uint_as_float(vv.y & 0xffff0000u);
    }
    const float il = 1.f / l;
    typedef float f32x2 __attribute__((ext_vector_type(2))); typedef __bf16 bf16x2 __attribute__((ext_vector_type(2)));
    const f32x2 lo = {a0 * il, a1 * il}, hi = {a2 * il, a3 * il};
    uint2 w; w.x = __builtin_bit_cast(unsigned, __builtin_convertvector(lo, bf16x2)); w.y = __builtin_bit_cast(unsigned, __builtin_convertvector(hi, bf16x2));
    *(uint2*)((char*)arg[3] + qo + r * 512) = w;
  }
}

extern "C" void kernel_launch(void* const* d_in, const int* in_sizes, int n_in, void* d_out, int out_size, void* d_ws, size_t ws_size,
                              hipStream_t stream) {
  standalone__mha_softmax__near_ridge_kernel<<<256, 256, 0, stream>>>(d_in[0], d_in[1], d_in[2], d_out);
}
```
